```python
import torch

def _softmax(x, axis=-1):
    return torch.softmax(x, dim=axis)
def _gelu(x):
    return torch.nn.functional.gelu(x, approximate="tanh")
def _silu(x):
    return torch.nn.functional.silu(x)
def _sigmoid(x):
    return torch.sigmoid(x)
def _layernorm(x, axis=-1, eps=1e-5):
    m = x.mean(dim=axis, keepdim=True)
    v = x.var(dim=axis, keepdim=True, unbiased=False)
    return (x - m) / torch.sqrt(v + eps)
def _scatter_add(tgt, idx, src, axis=0):
    return tgt.clone().index_add_(axis, idx, src)
def _segment_sum(src, seg, n):
    out = torch.zeros((n,) + src.shape[1:], dtype=src.dtype)
    return out.index_add_(0, seg, src)
def _pad(x, pw):
    flat = tuple(v for pair in reversed(pw) for v in pair)
    return torch.nn.functional.pad(x, flat)

kernel_name = 'concat_heads__compressed_kv_cla__sliding_block_cached__decode'

def setup_inputs():

    _w = (lambda a: (lambda t: t.to(torch.bfloat16) if t.is_floating_point() else t)(torch.as_tensor(a)))
    def _A_setup():
        B, H, Sq, Sk, D, M = 2, 32, 1, 8192, 128, 512
        Dc = 4 * D
        return {
            "x": _w((lambda *s: torch.randn(*s, dtype=torch.bfloat16) * 3.2)(B, Sq, M)),
            "c_kv": _w((lambda *s: torch.randn(*s, dtype=torch.bfloat16) * 0.1)(B, Sk, Dc)),
            "w_q": _w((lambda *s: torch.randn(*s, dtype=torch.bfloat16) * 6.4)(M, H, D) * (M ** -0.5)),
            "w_uk": _w((lambda *s: torch.randn(*s, dtype=torch.bfloat16) * 6.4)(Dc, H, D) * (Dc ** -0.5)),
            "w_uv": _w((lambda *s: torch.randn(*s, dtype=torch.bfloat16) * 0.1)(Dc, H, D) * (Dc ** -0.5)),
            "w_o": _w((lambda *s: torch.randn(*s, dtype=torch.bfloat16) * 0.1)(H, D, M) * ((H * D) ** -0.5)),
        }
    _A_in = _A_setup()

    def _B_setup():
        B, Sq, D = 2, 1, 512
        Hq, Hkv, Sk, Dh = 32, 32, 8192, 128
        assert Hq % Hkv == 0, (Hq, Hkv)
        return {
            "x": _w((lambda *s: torch.randn(*s, dtype=torch.bfloat16) * 1.6)(B, Sq, D)),
            "k_cache": _w((lambda *s: torch.randn(*s, dtype=torch.bfloat16) * 1.6)(B, Hkv, Sk, Dh)),
            "v_cache": _w((lambda *s: torch.randn(*s, dtype=torch.bfloat16) * 0.05)(B, Hkv, Sk, Dh)),
            "wq": _w((lambda *s: torch.randn(*s, dtype=torch.bfloat16) * 1.6)(D, Hq * Dh) * (D ** -0.5)),
            "wo": _w((lambda *s: torch.randn(*s, dtype=torch.bfloat16) * 0.05)(Hq * Dh, D) * ((Hq * Dh) ** -0.5)),
        }
    _B_in = _B_setup()

    return {
        "A_x": _A_in["x"],
        "A_c_kv": _A_in["c_kv"],
        "A_w_q": _A_in["w_q"],
        "A_w_uk": _A_in["w_uk"],
        "A_w_uv": _A_in["w_uv"],
        "A_w_o": _A_in["w_o"],
        "B_x": _B_in["x"],
        "B_k_cache": _B_in["k_cache"],
        "B_v_cache": _B_in["v_cache"],
        "B_wq": _B_in["wq"],
        "B_wo": _B_in["wo"],
    }

def reference(A_x, A_c_kv, A_w_q, A_w_uk, A_w_uv, A_w_o, B_x, B_k_cache, B_v_cache, B_wq, B_wo):
    def _A_ref(x, c_kv, w_q, w_uk, w_uv, w_o):
        B, Sq, M = x.shape[0], x.shape[1], x.shape[2]
        Sk = c_kv.shape[1]
        H, D = w_q.shape[1], w_q.shape[2]
        q = torch.einsum("bqm,mhd->bhqd", x, w_q)
        k = torch.einsum("bkc,chd->bhkd", c_kv, w_uk)
        v = torch.einsum("bkc,chd->bhkd", c_kv, w_uv)
        scores = torch.einsum("bhqd,bhkd->bhqk", q, k) * (D ** -0.5)
        off = Sk - Sq
        qi = torch.arange(Sq)[:, None]
        kj = torch.arange(Sk)[None, :]
        cmask = kj <= (qi + off)
        scores = torch.where(cmask[None, None], scores, -1e9)
        p = _softmax(scores, axis=-1)
        ctx = torch.einsum("bhqk,bhkd->bhqd", p, v)
        return torch.einsum("bhqd,hdm->bqm", ctx, w_o)

    def _B_ref(x, k_cache, v_cache, wq, wo):
        B, Sq, D = x.shape
        Hq, Dh = 32, 128
        Hkv, Sk = k_cache.shape[1], k_cache.shape[2]
        W = min(4096, max(256, Sk // 4))
        rep = Hq // Hkv
        q = (lambda x, *d: x.permute(*d))((x @ wq).reshape(B, Sq, Hq, Dh), 0, 2, 1, 3)
        k = (lambda x, n, axis: torch.repeat_interleave(x, n, dim=axis))(k_cache[:, :, Sk - W:, :], rep, axis=1)
        v = (lambda x, n, axis: torch.repeat_interleave(x, n, dim=axis))(v_cache[:, :, Sk - W:, :], rep, axis=1)
        scores = torch.einsum("bhqd,bhkd->bhqk", q, k) * (Dh ** -0.5)
        p = _softmax(scores, axis=-1)
        o = torch.einsum("bhqk,bhkd->bhqd", p, v)
        return (lambda x, *d: x.permute(*d))(o, 0, 2, 1, 3).reshape(B, Sq, Hq * Dh) @ wo

    a = _A_ref(A_x, A_c_kv, A_w_q, A_w_uk, A_w_uv, A_w_o)
    b = _B_ref(B_x, B_k_cache, B_v_cache, B_wq, B_wo)
    return torch.concatenate([a, b], axis=-1)
```

```cpp
#include <hip/hip_runtime.h>
#include <hip/hip_bf16.h>
#include <stdint.h>

typedef unsigned short u16;
__device__ __forceinline__ float bf2f(u16 v) { return __uint_as_float(((unsigned)v) << 16); }
__device__ __forceinline__ u16 f2bf(float f) { unsigned u = __float_as_uint(f); u += 0x7fffu + ((u >> 16) & 1u); return (u16)(u >> 16); }
__device__ __forceinline__ float rbf(float f) { return bf2f(f2bf(f)); }

constexpr int NB = 2, H = 32, SK = 8192, D = 128, M = 512, DC = 512, W = 2048;

__global__ void k_q(const u16* xA, const u16* wqA, const u16* xB, const u16* wqB, float* q) {
  int n = blockIdx.x * blockDim.x + threadIdx.x;
  int which = n >> 12; int col = n & 4095;
  const u16* x = which ? xB : xA; const u16* w = which ? wqB : wqA;
  float a0 = 0.f, a1 = 0.f;
  for (int m = 0; m < M; ++m) { float wv = bf2f(w[(size_t)m * 4096 + col]); a0 += bf2f(x[m]) * wv; a1 += bf2f(x[M + m]) * wv; }
  q[(which * 2 + 0) * 4096 + col] = rbf(a0);
  q[(which * 2 + 1) * 4096 + col] = rbf(a1);
}
__global__ void k_qlat(const u16* wuk, const float* q, float* qlat) {
  int wv = (blockIdx.x * blockDim.x + threadIdx.x) >> 6; int lane = threadIdx.x & 63;
  int c = wv >> 5, h = wv & 31;
  const u16* row = wuk + ((size_t)c * H + h) * D;
  float w0 = bf2f(row[2 * lane]), w1 = bf2f(row[2 * lane + 1]);
  float s0 = w0 * q[h * D + 2 * lane] + w1 * q[h * D + 2 * lane + 1];
  float s1 = w0 * q[4096 + h * D + 2 * lane] + w1 * q[4096 + h * D + 2 * lane + 1];
  for (int o = 32; o; o >>= 1) { s0 += __shfl_xor(s0, o); s1 += __shfl_xor(s1, o); }
  if (lane == 0) { qlat[(0 * H + h) * DC + c] = rbf(s0); qlat[(1 * H + h) * DC + c] = rbf(s1); }
}
__global__ void k_scoresA(const u16* ckv, const float* qlat, float* sA) {
  int wv = (blockIdx.x * blockDim.x + threadIdx.x) >> 6; int lane = threadIdx.x & 63;
  int b = wv >> 13, k = wv & 8191;
  const u16* row = ckv + (size_t)wv * DC + lane * 8;
  float v[8]; for (int j = 0; j < 8; ++j) v[j] = bf2f(row[j]);
  for (int h = 0; h < H; ++h) {
    const float* ql = qlat + (b * H + h) * DC + lane * 8;
    float s = 0.f; for (int j = 0; j < 8; ++j) s += v[j] * ql[j];
    for (int o = 32; o; o >>= 1) s += __shfl_xor(s, o);
    if (lane == 0) sA[((size_t)(b * H + h)) * SK + k] = rbf(rbf(s) * 0.08838834764831845f);
  }
}
__global__ void k_scoresB(const u16* kc, const float* q, float* sB) {
  int t = blockIdx.x * blockDim.x + threadIdx.x; int key = t >> 4; int l = t & 15;
  int bh = key >> 11, k = key & 2047; int b = bh >> 5, h = bh & 31;
  const u16* row = kc + ((size_t)bh * SK + (SK - W) + k) * D + l * 8;
  const float* qq = q + (2 + b) * 4096 + h * D + l * 8;
  float s = 0.f; for (int j = 0; j < 8; ++j) s += bf2f(row[j]) * qq[j];
  for (int o = 8; o; o >>= 1) s += __shfl_xor(s, o);
  if (l == 0) sB[key] = rbf(rbf(s) * 0.08838834764831845f);
}
__global__ void k_softmax(float* s, int n) {
  __shared__ float red[256];
  float* r = s + (size_t)blockIdx.x * n;
  float m = -INFINITY; for (int i = threadIdx.x; i < n; i += 256) m = fmaxf(m, r[i]);
  red[threadIdx.x] = m; __syncthreads();
  for (int o = 128; o; o >>= 1) { if (threadIdx.x < o) red[threadIdx.x] = fmaxf(red[threadIdx.x], red[threadIdx.x + o]); __syncthreads(); }
  m = red[0]; __syncthreads();
  float l = 0.f; for (int i = threadIdx.x; i < n; i += 256) { float e = __expf(r[i] - m); r[i] = e; l += e; }
  red[threadIdx.x] = l; __syncthreads();
  for (int o = 128; o; o >>= 1) { if (threadIdx.x < o) red[threadIdx.x] += red[threadIdx.x + o]; __syncthreads(); }
  float inv = 1.f / red[0];
  for (int i = threadIdx.x; i < n; i += 256) r[i] *= inv;
}
__global__ void k_pvA(const u16* ckv, const float* pA, float* part) {
  int bh = blockIdx.x, ks = blockIdx.y; int b = bh >> 5;
  int c = threadIdx.x * 2;
  float a0 = 0.f, a1 = 0.f;
  for (int k = ks * 512; k < ks * 512 + 512; ++k) {
    float p = pA[(size_t)bh * SK + k];
    unsigned v = *(const unsigned*)(ckv + ((size_t)b * SK + k) * DC + c);
    a0 += p * __uint_as_float(v << 16); a1 += p * __uint_as_float(v & 0xffff0000u);
  }
  part[((size_t)ks * 64 + bh) * DC + c] = a0; part[((size_t)ks * 64 + bh) * DC + c + 1] = a1;
}
__global__ void k_uv(const float* part, const u16* wuv, float* ctx) {
  __shared__ float cl[DC];
  int bh = blockIdx.x; int h = bh & 31;
  for (int c = threadIdx.x; c < DC; c += 128) { float s = 0.f; for (int ks = 0; ks < 16; ++ks) s += part[((size_t)ks * 64 + bh) * DC + c]; cl[c] = s; }
  __syncthreads();
  float a = 0.f; int d = threadIdx.x;
  for (int c = 0; c < DC; ++c) a += cl[c] * bf2f(wuv[((size_t)c * H + h) * D + d]);
  ctx[bh * D + d] = a;
}
__global__ void k_pvB(const u16* vc, const float* pB, float* ctx) {
  __shared__ float red[4][128];
  int bh = blockIdx.x; int d = threadIdx.x & 127, ks = threadIdx.x >> 7;
  float a = 0.f;
  for (int k = ks * 512; k < ks * 512 + 512; ++k) a += pB[bh * W + k] * bf2f(vc[((size_t)bh * SK + (SK - W) + k) * D + d]);
  red[ks][d] = a; __syncthreads();
  if (ks == 0) ctx[8192 + bh * D + d] = red[0][d] + red[1][d] + red[2][d] + red[3][d];
}
__global__ void k_out(const float* ctx, const u16* woA, const u16* woB, u16* out) {
  __shared__ float red[4][64];
  int wb = blockIdx.x; int which = wb >> 1, b = wb & 1;
  int n = blockIdx.y * 64 + (threadIdx.x & 63); int rs = threadIdx.x >> 6;
  const u16* w = which ? woB : woA;
  const float* cx = ctx + which * 8192 + b * 4096;
  float a = 0.f;
  for (int r = rs * 1024; r < rs * 1024 + 1024; ++r) a += cx[r] * bf2f(w[(size_t)r * 512 + n]);
  red[rs][threadIdx.x & 63] = a; __syncthreads();
  if (rs == 0) { int t = threadIdx.x; out[b * 1024 + which * 512 + n] = f2bf(red[0][t] + red[1][t] + red[2][t] + red[3][t]); }
}

extern "C" void kernel_launch(void* const* d_in, const int* in_sizes, int n_in,
                              void* d_out, int out_size, void* d_ws, size_t ws_size,
                              hipStream_t stream) {
  const u16* xA = (const u16*)d_in[0]; const u16* ckv = (const u16*)d_in[1]; const u16* wqA = (const u16*)d_in[2];
  const u16* wuk = (const u16*)d_in[3]; const u16* wuv = (const u16*)d_in[4]; const u16* woA = (const u16*)d_in[5];
  const u16* xB = (const u16*)d_in[6]; const u16* kc = (const u16*)d_in[7]; const u16* vc = (const u16*)d_in[8];
  const u16* wqB = (const u16*)d_in[9]; const u16* woB = (const u16*)d_in[10];
  float* ws = (float*)d_ws;
  float* q = ws;
  float* qlat = q + 16384;
  float* sA = qlat + 32768;
  float* sB = sA + 64 * 8192;
  float* part = sB + 64 * 2048;
  float* ctx = part + 16 * 64 * 512;
  k_q<<<32, 256, 0, stream>>>(xA, wqA, xB, wqB, q);
  k_qlat<<<4096, 256, 0, stream>>>(wuk, q, qlat);
  k_scoresA<<<4096, 256, 0, stream>>>(ckv, qlat, sA);
  k_scoresB<<<64 * 2048 * 16 / 256, 256, 0, stream>>>(kc, q, sB);
  k_softmax<<<64, 256, 0, stream>>>(sA, SK);
  k_softmax<<<64, 256, 0, stream>>>(sB, W);
  k_pvA<<<dim3(64, 16), 256, 0, stream>>>(ckv, sA, part);
  k_uv<<<64, 128, 0, stream>>>(part, wuv, ctx);
  k_pvB<<<64, 512, 0, stream>>>(vc, sB, ctx);
  k_out<<<dim3(4, 8), 256, 0, stream>>>(ctx, woA, woB, (u16*)d_out);
}
```

```cpp
#include <hip/hip_runtime.h>
#include <stdint.h>
#define SHPOL " nt"

typedef unsigned short u16;
typedef unsigned long long u64;
typedef unsigned v4u __attribute__((ext_vector_type(4)));
typedef unsigned v2u __attribute__((ext_vector_type(2)));
typedef float f32x4 __attribute__((ext_vector_type(4)));
typedef float f32x16 __attribute__((ext_vector_type(16)));
typedef short bf16x8 __attribute__((ext_vector_type(8)));
typedef short s16x4 __attribute__((ext_vector_type(4)));
typedef __bf16 bf16x2 __attribute__((ext_vector_type(2)));
#define LAS __attribute__((address_space(3)))
#define DI __device__ __forceinline__

constexpr unsigned WS_EPOCH = 0;
constexpr unsigned WS_Q1 = 4096;
constexpr unsigned WS_QL = WS_Q1 + 65536u * 8;
constexpr unsigned WS_ML = WS_QL + 16384u * 8;
constexpr unsigned WS_C4 = WS_ML + 8192u * 8;
constexpr unsigned WS_REC = WS_C4 + 32768u * 8;
constexpr unsigned WS_Y = WS_REC + 32u * 4 * 136 * 8;
constexpr unsigned WS_PART = WS_Y + 65536u * 8;
constexpr unsigned WS_STAMP = WS_PART + 128u * 32 * 512 * 2;
constexpr unsigned WS_END = WS_STAMP + 256u * 16 * 8;

constexpr int LDS_BYTES = 163840;
constexpr unsigned SCR = 131072;
constexpr unsigned SPIN_CAP = 1u << 16;
#define KINIT_ 1
#define KWIN_ 8
#define CK1_ 12
#define VPRE_ 8
#define KSLOW_ 3
#define SWIN_ 0
#define CK0_ 6
constexpr int CK0 = CK0_;
constexpr int KSLOW = KSLOW_, SWIN = SWIN_;
constexpr int KINIT = KINIT_, KWIN = KWIN_, CK1 = CK1_, VPRE = VPRE_;
constexpr float SCALE = 0.08838834764831845f;
constexpr float LOG2E = 1.4426950408889634f;

struct Params {
  const u16 *xA, *ckv, *wqA, *wuk, *wuv, *woA, *xB, *kc, *vc, *wqB, *woB;
  u16* out;
  unsigned char* ws;
};

DI float bflo(unsigned w) { return __uint_as_float(w << 16); }
DI float bfhi(unsigned w) { return __uint_as_float(w & 0xffff0000u); }
DI unsigned f2bf_bits(float f) { return (unsigned)__builtin_bit_cast(unsigned short, (__bf16)f); }
DI float rbf(float f) { return __uint_as_float(f2bf_bits(f) << 16); }
DI unsigned pk_bf(float lo, float hi) { return f2bf_bits(lo) | (f2bf_bits(hi) << 16); }
DI float dot2(unsigned a, unsigned b, float acc) {
  return __builtin_amdgcn_fdot2_f32_bf16(__builtin_bit_cast(bf16x2, a), __builtin_bit_cast(bf16x2, b), acc, false);
}
template <int CTRL> DI float dpp(float x) {
  return __builtin_bit_cast(float, __builtin_amdgcn_mov_dpp(__builtin_bit_cast(int, x), CTRL, 0xf, 0xf, true));
}
DI float row16_sum(float x) { x += dpp<0xB1>(x); x += dpp<0x4E>(x); x += dpp<0x141>(x); x += dpp<0x140>(x); return x; }
DI float xrow_max(float x) {
  auto s = __builtin_amdgcn_permlane16_swap(__float_as_uint(x), __float_as_uint(x), false, false);
  x = fmaxf(__uint_as_float(s[0]), __uint_as_float(s[1]));
  auto t = __builtin_amdgcn_permlane32_swap(__float_as_uint(x), __float_as_uint(x), false, false);
  return fmaxf(__uint_as_float(t[0]), __uint_as_float(t[1]));
}
DI float xrow_sum(float x) {
  auto s = __builtin_amdgcn_permlane16_swap(__float_as_uint(x), __float_as_uint(x), false, false);
  x = __uint_as_float(s[0]) + __uint_as_float(s[1]);
  auto t = __builtin_amdgcn_permlane32_swap(__float_as_uint(x), __float_as_uint(x), false, false);
  return __uint_as_float(t[0]) + __uint_as_float(t[1]);
}
DI float swap32_add(float a, float b) {
  auto t = __builtin_amdgcn_permlane32_swap(__float_as_uint(a), __float_as_uint(b), false, false);
  return __uint_as_float(t[0]) + __uint_as_float(t[1]);
}
DI float swap16_add(float a, float b) {
  auto t = __builtin_amdgcn_permlane16_swap(__float_as_uint(a), __float_as_uint(b), false, false);
  return __uint_as_float(t[0]) + __uint_as_float(t[1]);
}
DI v4u ldnt(const u16* p) { return __builtin_nontemporal_load(reinterpret_cast<const v4u*>(p)); }
DI void wg_barrier() { asm volatile("s_waitcnt lgkmcnt(0)\n\ts_barrier" ::: "memory"); }
DI void glds16_nt(const void* gsrc, unsigned lds_dst) {
  unsigned keep;
  asm volatile("s_mov_b32 %0, m0\n\ts_mov_b32 m0, %2\n\ts_nop 0\n\tglobal_load_lds_dwordx4 %1, off nt\n\ts_mov_b32 m0, %0"
               : "=&s"(keep) : "v"(gsrc), "s"(lds_dst) : "memory");
}
DI void glds16_def(const void* gsrc, unsigned lds_dst) {
  unsigned keep;
  asm volatile("s_mov_b32 %0, m0\n\ts_mov_b32 m0, %2\n\ts_nop 0\n\tglobal_load_lds_dwordx4 %1, off" SHPOL "\n\ts_mov_b32 m0, %0"
               : "=&s"(keep) : "v"(gsrc), "s"(lds_dst) : "memory");
}
typedef __amdgpu_buffer_rsrc_t rsrc_t;
DI rsrc_t mk_rsrc(const void* base, unsigned bytes) {
  const u64 b = (u64)base;
  const unsigned lo = __builtin_amdgcn_readfirstlane((unsigned)b), hi = __builtin_amdgcn_readfirstlane((unsigned)(b >> 32));
  return __builtin_amdgcn_make_buffer_rsrc((void*)(((u64)hi << 32) | lo), 0, bytes, 0x00020000);
}
DI v4u ldnt_b(rsrc_t r, unsigned voff, unsigned soff) { return __builtin_amdgcn_raw_buffer_load_b128(r, voff, soff, 2); }
DI v4u ld16(rsrc_t r, unsigned off) { return __builtin_amdgcn_raw_buffer_load_b128(r, off, 0, 16); }
DI void st8(rsrc_t r, unsigned off, unsigned lo, unsigned hi) { v2u v = {lo, hi}; __builtin_amdgcn_raw_buffer_store_b64(v, r, off, 0, 16); }
DI void st16(rsrc_t r, unsigned off, v4u v) { __builtin_amdgcn_raw_buffer_store_b128(v, r, off, 0, 16); }

struct Tag { unsigned hi, lo8, t32, top; };
DI unsigned g_lo(float v, const Tag& t) { return ((__float_as_uint(v) + 0x80u) >> 8) | (t.lo8 << 24); }
DI float g_val(unsigned lo) { return __uint_as_float(lo << 8); }
DI bool g_ok(unsigned lo, unsigned hi, const Tag& t) { return hi == t.hi && (lo >> 24) == t.lo8; }
DI bool g_ok2(const v4u& v, const Tag& t) { return g_ok(v.x, v.y, t) && g_ok(v.z, v.w, t); }
DI float round24(float v) { return __uint_as_float((__float_as_uint(v) + 0x80u) & 0xffffff00u); }
DI unsigned swz(unsigned r) { return ((r & 3u) << 2) | ((r >> 2) & 3u); }

#define STAMP(k) do { } while (0)

template <typename T> DI void lds_st(unsigned addr, T v) { *(LAS T*)(uintptr_t)addr = v; }
template <typename T> DI T lds_ld(unsigned addr) { return *(LAS T*)(uintptr_t)addr; }

typedef float f32x4v __attribute__((ext_vector_type(4)));

DI void dma_slice(const u16* src0, size_t row_stride, unsigned dst, int w, int lane) {
  asm volatile("" : "+v"(lane));
  const int g = lane >> 4, c16 = lane & 15;
  const u16* s = src0 + (size_t)(16 * w + g) * row_stride;
#pragma unroll
  for (int i = 0; i < 4; ++i) glds16_nt(s + (size_t)(4 * i) * row_stride + 8 * (c16 ^ ((g << 2) | i)), dst + (16 * w + 4 * i) * 256);
}
DI f32x4v gemv_mfma(const bf16x8 (&a)[4], unsigned wbase, int w, int lane) {
  const unsigned g = lane >> 4, q = (lane & 15) >> 2, pp = lane & 3;
  const unsigned chn = 2 * w + (pp >> 1);
  const unsigned a0 = wbase + (8 * g + q) * 256 + 16 * (chn ^ ((q << 2) | ((2 * g) & 3))) + 8 * (pp & 1);
  const unsigned a1 = wbase + (8 * g + 4 + q) * 256 + 16 * (chn ^ ((q << 2) | ((2 * g + 1) & 3))) + 8 * (pp & 1);
  s16x4 x[8];
  asm volatile("ds_read_b64_tr_b16 %0, %8\n\tds_read_b64_tr_b16 %1, %9\n\t"
               "ds_read_b64_tr_b16 %2, %8 offset:8192\n\tds_read_b64_tr_b16 %3, %9 offset:8192\n\t"
               "ds_read_b64_tr_b16 %4, %8 offset:16384\n\tds_read_b64_tr_b16 %5, %9 offset:16384\n\t"
               "ds_read_b64_tr_b16 %6, %8 offset:24576\n\tds_read_b64_tr_b16 %7, %9 offset:24576\n\ts_waitcnt lgkmcnt(0)"
               : "=&v"(x[0]), "=&v"(x[1]), "=&v"(x[2]), "=&v"(x[3]), "=&v"(x[4]), "=&v"(x[5]), "=&v"(x[6]), "=&v"(x[7])
               : "v"(a0), "v"(a1) : "memory");
  f32x4v d = {0.f, 0.f, 0.f, 0.f};
#pragma unroll
  for (int s = 0; s < 4; ++s)
    d = __builtin_amdgcn_mfma_f32_16x16x32_bf16(a[s], __builtin_shufflevector(x[2 * s], x[2 * s + 1], 0, 1, 2, 3, 4, 5, 6, 7), d, 0, 0, 0);
  return d;
}

template <int I0, int I1, bool NT = true> DI void dma_rows1k(const u16* src, unsigned dst, int w, int lane) {
  asm volatile("" : "+v"(lane));
  const u16* s = src + (size_t)(16 * w) * 512;
#pragma unroll
  for (int i = I0; i < I1; ++i) {
    if (NT) glds16_nt(s + i * 512 + 8 * (lane ^ swz(i)), dst + (16 * w + i) * 1024);
    else glds16_def(s + i * 512 + 8 * (lane ^ swz(i)), dst + (16 * w + i) * 1024);
  }
}
DI void vec_frags(bf16x8 (&a)[4], unsigned vbase, int lane) {
  const unsigned ad = vbase + (lane & 1) * 256 + (lane >> 4) * 16;
#pragma unroll
  for (int s = 0; s < 4; ++s) a[s] = lds_ld<bf16x8>(ad + 64 * s);
}
DI void gemv_publish(rsrc_t rs, const Tag& tg, const f32x4v& d, unsigned base, unsigned bstride, int w, int lane) {
  if (lane < 32) {
    const float v = lane < 16 ? d[0] : d[1];
    st8(rs, base + ((lane >> 4) * bstride + 16 * w + (lane & 15)) * 8, g_lo(v, tg), tg.hi);
  }
}
DI unsigned gather4(rsrc_t rs, const Tag& tg, unsigned off, unsigned pstride) {
  v4u v[4];
#pragma unroll
  for (int pj = 0; pj < 4; ++pj) v[pj] = ld16(rs, off + pj * pstride);
  unsigned spins = 0;
  for (;;) {
    bool bad[4], any = false;
#pragma unroll
    for (int pj = 0; pj < 4; ++pj) { bad[pj] = !__all(g_ok2(v[pj], tg)); any = any || bad[pj]; }
    if (!any || ++spins >= SPIN_CAP) break;
    asm volatile("" ::: "memory");
#pragma unroll
    for (int pj = 0; pj < 4; ++pj) if (bad[pj]) v[pj] = ld16(rs, off + pj * pstride);
  }
  return pk_bf((g_val(v[0].x) + g_val(v[1].x)) + (g_val(v[2].x) + g_val(v[3].x)), (g_val(v[0].z) + g_val(v[1].z)) + (g_val(v[2].z) + g_val(v[3].z)));
}
DI Tag make_tag(u64 E) {
  const u64 T = E + 1;
  Tag tg;
  tg.hi = __builtin_amdgcn_readfirstlane((unsigned)(T >> 8));
  tg.lo8 = __builtin_amdgcn_readfirstlane((unsigned)T & 0xffu);
  tg.t32 = __builtin_amdgcn_readfirstlane((unsigned)T);
  tg.top = __builtin_amdgcn_readfirstlane((unsigned)(T >> 32));
  return tg;
}

DI void stream_wg(const Params& p, rsrc_t rs, const u64 E, int h, int j, unsigned lb) {
  const int b = j >> 1, half = j & 1, bh = b * 32 + h;
  const int tid = threadIdx.x, lane = tid & 63, w = __builtin_amdgcn_readfirstlane(tid >> 6), c16 = lane & 15;
  const size_t off = ((size_t)bh * 8192 + 6144 + half * 1024 + w * 128) * 128;
  const rsrc_t rk = mk_rsrc(p.kc + off, 32768), rv = mk_rsrc(p.vc + off, 32768);
  dma_slice(p.wqB + (size_t)(128 * j) * 4096 + h * 128, 4096, lb + 8192, w, lane);
  bf16x8 xa[4];
#pragma unroll
  for (int s = 0; s < 4; ++s) xa[s] = *reinterpret_cast<const bf16x8*>(p.xB + (lane & 1) * 512 + 128 * j + 32 * s + 8 * (lane >> 4));
  v4u K[32], V[32];
#pragma unroll
  for (int i = 0; i < KINIT; ++i) K[i] = ldnt_b(rk, lane * 16, i * 1024);
  asm volatile("s_waitcnt vmcnt(%0)" :: "i"(KINIT) : "memory");
  wg_barrier();
  const Tag tg = make_tag(E);
  {
    const f32x4v d = gemv_mfma(xa, lb + 8192, w, lane);
    gemv_publish(rs, tg, d, WS_Q1 + (((unsigned)h * 4 + j) * 2 + 1) * 2048, 128, w, lane);
  }
  STAMP(1);
  v4u qa[4];
  const unsigned qoff = WS_Q1 + ((((unsigned)h * 4) * 2 + 1) * 2 + b) * 1024 + lane * 16;
#pragma unroll
  for (int i = KINIT; i < 32 + VPRE; ++i) {
    if (i < 32) K[i] = ldnt_b(rk, lane * 16, i * 1024); else V[i - 32] = ldnt_b(rv, lane * 16, (i - 32) * 1024);
    if (i == 24) { if (w == 0) {
#pragma unroll
        for (int pj = 0; pj < 4; ++pj) qa[pj] = ld16(rs, qoff + pj * 4096); } }
    if (i < KINIT + KSLOW) asm volatile("s_waitcnt vmcnt(%0)" :: "i"(SWIN) : "memory");
    else if (i >= KWIN) asm volatile("s_waitcnt vmcnt(%0)" :: "i"(KWIN) : "memory");
  }
  if (w == 0) {
    unsigned spins = 0;
    while (!__all(g_ok2(qa[0], tg) && g_ok2(qa[1], tg) && g_ok2(qa[2], tg) && g_ok2(qa[3], tg)) && ++spins < SPIN_CAP) {
      asm volatile("" ::: "memory");
#pragma unroll
      for (int pj = 0; pj < 4; ++pj) qa[pj] = ld16(rs, qoff + pj * 4096);
    }
    const float q0 = (g_val(qa[0].x) + g_val(qa[1].x)) + (g_val(qa[2].x) + g_val(qa[3].x));
    const float q1 = (g_val(qa[0].z) + g_val(qa[1].z)) + (g_val(qa[2].z) + g_val(qa[3].z));
    lds_st<unsigned>(lb + lane * 4, pk_bf(q0, q1));
  }
  wg_barrier();
  STAMP(2);
  const v4u q = lds_ld<v4u>(lb + c16 * 16);
  float s[32];
#pragma unroll
  for (int i = 0; i < 32; ++i) {
    float d = dot2(K[i].x, q.x, 0.f); d = dot2(K[i].y, q.y, d); d = dot2(K[i].z, q.z, d); d = dot2(K[i].w, q.w, d);
    if (i + VPRE < 32) V[i + VPRE] = ldnt_b(rv, lane * 16, (i + VPRE) * 1024);
    s[i] = d;
  }
  float m = -INFINITY;
#pragma unroll
  for (int i = 0; i < 32; ++i) { s[i] = rbf(rbf(row16_sum(s[i])) * SCALE); m = fmaxf(m, s[i]); }
  m = xrow_max(m);
  float l = 0.f, acc[8];
#pragma unroll
  for (int k = 0; k < 8; ++k) acc[k] = 0.f;
  const float mneg = -m * LOG2E;
#pragma unroll
  for (int i = 0; i < 32; ++i) { s[i] = __builtin_amdgcn_exp2f(fmaf(s[i], LOG2E, mneg)); l += s[i]; }
  l = xrow_sum(l);
  const unsigned ST = lb + 512, mg = lb + 1024;
  if (lane == 0) lds_st<v2u>(ST + w * 8, v2u{__float_as_uint(m), __float_as_uint(l)});
  wg_barrier();
  float M, L;
  {
    const f32x4 s0 = lds_ld<f32x4>(ST), s1 = lds_ld<f32x4>(ST + 16), s2 = lds_ld<f32x4>(ST + 32), s3 = lds_ld<f32x4>(ST + 48);
    M = fmaxf(fmaxf(fmaxf(s0[0], s0[2]), fmaxf(s1[0], s1[2])), fmaxf(fmaxf(s2[0], s2[2]), fmaxf(s3[0], s3[2])));
    L = __builtin_amdgcn_exp2f((s0[0] - M) * LOG2E) * s0[1] + __builtin_amdgcn_exp2f((s0[2] - M) * LOG2E) * s0[3]
      + __builtin_amdgcn_exp2f((s1[0] - M) * LOG2E) * s1[1] + __builtin_amdgcn_exp2f((s1[2] - M) * LOG2E) * s1[3]
      + __builtin_amdgcn_exp2f((s2[0] - M) * LOG2E) * s2[1] + __builtin_amdgcn_exp2f((s2[2] - M) * LOG2E) * s2[3]
      + __builtin_amdgcn_exp2f((s3[0] - M) * LOG2E) * s3[1] + __builtin_amdgcn_exp2f((s3[2] - M) * LOG2E) * s3[3];
  }
  const float ew = __builtin_amdgcn_exp2f((m - M) * LOG2E);
  if (tid == 0) st16(rs, WS_REC + (((unsigned)h * 4 + j) * 136 + 128) * 8, v4u{g_lo(L, tg), tg.hi, g_lo(M, tg), tg.hi});
  STAMP(3);
#pragma unroll
  for (int i = 0; i < 32; ++i) {
#pragma unroll
    for (int k = 0; k < 4; ++k) { acc[2 * k] = fmaf(s[i], bflo(V[i][k]), acc[2 * k]); acc[2 * k + 1] = fmaf(s[i], bfhi(V[i][k]), acc[2 * k + 1]); }
  }
  float r[4], t[2];
#pragma unroll
  for (int k = 0; k < 4; ++k) r[k] = swap32_add(acc[k], acc[k + 4]);
#pragma unroll
  for (int k = 0; k < 2; ++k) t[k] = swap16_add(r[k], r[k + 2]) * ew;
  {
    const int g = lane >> 4;
    lds_st<v2u>(mg + w * 512 + (8 * c16 + 4 * (g >> 1) + 2 * (g & 1)) * 4, v2u{__float_as_uint(t[0]), __float_as_uint(t[1])});
  }
  wg_barrier();
  STAMP(4);
  if (tid < 128) {
    float o = 0.f;
#pragma unroll
    for (int ww = 0; ww < 8; ++ww) o += lds_ld<float>(mg + ww * 512 + tid * 4);
    st8(rs, WS_REC + (((unsigned)h * 4 + j) * 136 + tid) * 8, g_lo(o, tg), tg.hi);
  }
  STAMP(5);
}

DI v4u poll_pair(rsrc_t rs, unsigned off, const Tag& tg) {
  v4u v = ld16(rs, off);
  unsigned spins = 0;
  while (!__all(g_ok2(v, tg)) && ++spins < SPIN_CAP) { __builtin_amdgcn_s_sleep(1); asm volatile("" ::: "memory"); v = ld16(rs, off); }
  return v;
}

DI void final_reduce(const Params& p, rsrc_t rs, const Tag& tg, int which, int a, int lane) {
  const int b = a >> 6, n0 = (a & 63) * 8, head = lane >> 1, hf = lane & 1;
  const unsigned off = WS_Y + ((((unsigned)which * 32 + head) * 2 + b) * 512 + n0 + 4 * hf) * 8;
  v4u v0 = ld16(rs, off), v1 = ld16(rs, off + 16);
  unsigned spins = 0;
  while (!(g_ok2(v0, tg) && g_ok2(v1, tg)) && ++spins < SPIN_CAP) { __builtin_amdgcn_s_sleep(1); asm volatile("" ::: "memory"); v0 = ld16(rs, off); v1 = ld16(rs, off + 16); }
  float r0 = swap32_add(g_val(v0.x), g_val(v1.x)), r1 = swap32_add(g_val(v0.z), g_val(v1.z));
  float x = swap16_add(r0, r1);
  x += dpp<0x4E>(x);
  x += dpp<0x124>(x);
  x += dpp<0x128>(x);
  if ((lane & 15) < 2) p.out[b * 1024 + which * 512 + n0 + 4 * hf + (lane >> 4)] = (u16)f2bf_bits(x);
}

DI void chain_wg(const Params& p, rsrc_t rs, const u64 E, int a, unsigned lb) {
  const int h = a >> 2, j = a & 3;
  const int tid = threadIdx.x, lane = tid & 63, w = __builtin_amdgcn_readfirstlane(tid >> 6), g = lane >> 4, c16 = lane & 15;
  const unsigned scr = lb + SCR;
  dma_slice(p.wqA + (size_t)(128 * j) * 4096 + h * 128, 4096, scr, w, lane);
  bf16x8 fa[4], WK[4];
#pragma unroll
  for (int s = 0; s < 4; ++s) fa[s] = *reinterpret_cast<const bf16x8*>(p.xA + (lane & 1) * 512 + 128 * j + 32 * s + 8 * g);
  {
    const rsrc_t rw = mk_rsrc(p.wuk + (size_t)(128 * j + 16 * w) * 4096 + h * 128, 16 * 8192);
#pragma unroll
    for (int s = 0; s < 4; ++s) WK[s] = __builtin_bit_cast(bf16x8, ldnt_b(rw, c16 * 8192 + g * 16, 64 * s));
  }
  const int bA = a >> 6;
  asm volatile("s_barrier" ::: "memory");
  dma_rows1k<0, CK0>(p.ckv + (size_t)a * 128 * 512, lb, w, lane);
  asm volatile("s_waitcnt vmcnt(%0)" :: "i"(4 + CK0) : "memory");
  wg_barrier();
  const Tag tg = make_tag(E);
  {
    const f32x4v d = gemv_mfma(fa, scr, w, lane);
    gemv_publish(rs, tg, d, WS_Q1 + (((unsigned)h * 4 + j) * 2 + 0) * 2048, 128, w, lane);
  }
  STAMP(1);
  dma_rows1k<CK0, CK1>(p.ckv + (size_t)a * 128 * 512, lb, w, lane);
  wg_barrier();
  if (tid < 128) {
    const unsigned v = gather4(rs, tg, WS_Q1 + (((unsigned)h * 4) * 4 + (tid >> 6)) * 1024 + (tid & 63) * 16, 4096);
    lds_st<unsigned>(scr + tid * 4, v);
  }
  wg_barrier();
  STAMP(2);
  {
    bf16x8 qf[4];
    vec_frags(qf, scr, lane);
    f32x4v d = {0.f, 0.f, 0.f, 0.f};
#pragma unroll
    for (int s = 0; s < 4; ++s) d = __builtin_amdgcn_mfma_f32_16x16x32_bf16(WK[s], qf[s], d, 0, 0, 0);
    if (c16 < 2)
      st16(rs, WS_QL + (((unsigned)c16 * 32 + h) * 256 + 64 * j + 8 * w + 2 * g) * 8, v4u{pk_bf(d[0], d[1]), tg.t32, pk_bf(d[2], d[3]), tg.t32});
  }
  STAMP(3);
  dma_rows1k<CK1, 16>(p.ckv + (size_t)a * 128 * 512, lb, w, lane);
  const int r32 = lane & 31, hh = lane >> 5;
  float sc[8];
  s16x4 xt[32];
  bf16x8 ka[16];
  const unsigned fr = swz(r32);
  {
    v4u v[8];
    const unsigned base = WS_QL + (unsigned)bA * 65536 + tid * 16;
#pragma unroll
    for (int i = 0; i < 8; ++i) { v[i] = ld16(rs, base + i * 8192); }
    asm volatile("s_waitcnt vmcnt(8)" ::: "memory");
    wg_barrier();
    STAMP(11);
    {
      const unsigned abase = lb + (32 * (w & 3) + r32) * 1024 + 512 * (w >> 2);
#pragma unroll
      for (int s = 0; s < 16; ++s) ka[s] = lds_ld<bf16x8>(abase + 16 * ((unsigned)((2 * s + hh) & 15) ^ fr) + 256 * (s >> 3));
      const unsigned blk = (lane >> 4) & 1, q = (lane & 15) >> 2, pp = lane & 3;
      unsigned tb[4];
#pragma unroll
      for (int mt = 0; mt < 2; ++mt)
#pragma unroll
        for (int t = 0; t < 2; ++t) {
          const unsigned row = 8 * hh + 4 * t + q, chn = 8 * w + 4 * mt + 2 * blk + (pp >> 1);
          tb[mt * 2 + t] = lb + row * 1024 + 16 * (chn ^ swz(row)) + 8 * (pp & 1);
        }
#pragma unroll
      for (int hf = 0; hf < 2; ++hf) {
        asm volatile(
            "ds_read_b64_tr_b16 %0, %16\n\tds_read_b64_tr_b16 %1, %17\n\tds_read_b64_tr_b16 %2, %18\n\tds_read_b64_tr_b16 %3, %19\n\t"
            "ds_read_b64_tr_b16 %4, %16 offset:16384\n\tds_read_b64_tr_b16 %5, %17 offset:16384\n\tds_read_b64_tr_b16 %6, %18 offset:16384\n\tds_read_b64_tr_b16 %7, %19 offset:16384\n\t"
            "ds_read_b64_tr_b16 %8, %16 offset:32768\n\tds_read_b64_tr_b16 %9, %17 offset:32768\n\tds_read_b64_tr_b16 %10, %18 offset:32768\n\tds_read_b64_tr_b16 %11, %19 offset:32768\n\t"
            "ds_read_b64_tr_b16 %12, %16 offset:49152\n\tds_read_b64_tr_b16 %13, %17 offset:49152\n\tds_read_b64_tr_b16 %14, %18 offset:49152\n\tds_read_b64_tr_b16 %15, %19 offset:49152\n\t"
            "s_waitcnt lgkmcnt(0)"
            : "=&v"(xt[16 * hf + 0]), "=&v"(xt[16 * hf + 1]), "=&v"(xt[16 * hf + 2]), "=&v"(xt[16 * hf + 3]),
              "=&v"(xt[16 * hf + 4]), "=&v"(xt[16 * hf + 5]), "=&v"(xt[16 * hf + 6]), "=&v"(xt[16 * hf + 7]),
              "=&v"(xt[16 * hf + 8]), "=&v"(xt[16 * hf + 9]), "=&v"(xt[16 * hf + 10]), "=&v"(xt[16 * hf + 11]),
              "=&v"(xt[16 * hf + 12]), "=&v"(xt[16 * hf + 13]), "=&v"(xt[16 * hf + 14]), "=&v"(xt[16 * hf + 15])
            : "v"(tb[0] + hf * 65536), "v"(tb[1] + hf * 65536), "v"(tb[2] + hf * 65536), "v"(tb[3] + hf * 65536) : "memory");
      }
    }
    STAMP(12);
    unsigned spins = 0;
    const unsigned cq = tid & 127, h0 = tid >> 7;
    for (;;) {
      bool bad[8], any = false;
#pragma unroll
      for (int i = 0; i < 8; ++i) { bad[i] = !__all(v[i].y == tg.t32 && v[i].w == tg.t32); any = any || bad[i]; }
      if (!any || ++spins >= SPIN_CAP) break;
      asm volatile("" ::: "memory");
#pragma unroll
      for (int i = 0; i < 8; ++i) if (bad[i]) v[i] = ld16(rs, base + i * 8192);
    }
#pragma unroll
    for (int i = 0; i < 8; ++i) {
      const unsigned head = 4 * i + h0;
      lds_st<v2u>(scr + head * 1024 + 16 * ((cq >> 1) ^ swz(head)) + 8 * (cq & 1), v2u{v[i].x, v[i].z});
    }
  }
  wg_barrier();
  STAMP(4);
  dma_slice(p.wuv + (size_t)(128 * j) * 4096 + h * 128, 4096, lb, w, lane);
  {
    const int ch = w >> 2;
    f32x16 acc;
#pragma unroll
    for (int i = 0; i < 16; ++i) acc[i] = 0.f;
    const unsigned bbase = scr + r32 * 1024 + 512 * ch;
#pragma unroll
    for (int s = 0; s < 16; ++s) {
      const bf16x8 bv = lds_ld<bf16x8>(bbase + 16 * ((unsigned)((2 * s + hh) & 15) ^ fr) + 256 * (s >> 3));
      acc = __builtin_amdgcn_mfma_f32_32x32x16_bf16(ka[s], bv, acc, 0, 0, 0);
      if ((s & 7) == 7) __builtin_amdgcn_sched_barrier(0);
    }
    f32x4 s0, s1;
#pragma unroll
    for (int k = 0; k < 4; ++k) { s0[k] = ch ? acc[k] : acc[8 + k]; s1[k] = ch ? acc[4 + k] : acc[12 + k]; }
    const unsigned XB = lb + 32768;
    lds_st<f32x4>(XB + w * 2048 + lane * 16, s0);
    lds_st<f32x4>(XB + w * 2048 + 1024 + lane * 16, s1);
    wg_barrier();
    const f32x4 t0 = lds_ld<f32x4>(XB + (w ^ 4) * 2048 + lane * 16), t1 = lds_ld<f32x4>(XB + (w ^ 4) * 2048 + 1024 + lane * 16);
#pragma unroll
    for (int k = 0; k < 4; ++k) { sc[k] = ((ch ? acc[8 + k] : acc[k]) + t0[k]) * SCALE; sc[4 + k] = ((ch ? acc[12 + k] : acc[4 + k]) + t1[k]) * SCALE; }
  }
  const unsigned MX = scr + 16384, LS = scr + 17408, PB = scr + 18432;
  float Mh;
  {
    float m = sc[0];
#pragma unroll
    for (int k = 1; k < 8; ++k) m = fmaxf(m, sc[k]);
    { auto t = __builtin_amdgcn_permlane32_swap(__float_as_uint(m), __float_as_uint(m), false, false); m = fmaxf(__uint_as_float(t[0]), __uint_as_float(t[1])); }
    if (lane < 32) lds_st<float>(MX + (lane * 8 + w) * 4, m);
    wg_barrier();
    const f32x4 ma = lds_ld<f32x4>(MX + r32 * 32), mb = lds_ld<f32x4>(MX + r32 * 32 + 16);
    float M = fmaxf(fmaxf(fmaxf(ma[0], ma[1]), fmaxf(ma[2], ma[3])), fmaxf(fmaxf(mb[0], mb[1]), fmaxf(mb[2], mb[3])));
    M = round24(M);
    Mh = M;
    const float mneg = -M * LOG2E;
    unsigned pk[4]; float l = 0.f;
#pragma unroll
    for (int k = 0; k < 4; ++k) {
      const float p0 = __builtin_amdgcn_exp2f(fmaf(sc[2 * k], LOG2E, mneg)), p1 = __builtin_amdgcn_exp2f(fmaf(sc[2 * k + 1], LOG2E, mneg));
      pk[k] = pk_bf(p0, p1);
      l += bflo(pk[k]) + bfhi(pk[k]);
    }
    { auto t = __builtin_amdgcn_permlane32_swap(__float_as_uint(l), __float_as_uint(l), false, false); l = __uint_as_float(t[0]) + __uint_as_float(t[1]); }
    if (lane < 32) lds_st<float>(LS + (lane * 8 + w) * 4, l);
    const int kt = w & 3, ch = w >> 2;
    lds_st<v2u>(PB + r32 * 272 + (32 * kt + 16 * ch + 4 * hh) * 2, v2u{pk[0], pk[1]});
    lds_st<v2u>(PB + r32 * 272 + (32 * kt + 16 * ch + 8 + 4 * hh) * 2, v2u{pk[2], pk[3]});
  }
  wg_barrier();
  f32x16 o0, o1;
#pragma unroll
  for (int i = 0; i < 16; ++i) { o0[i] = 0.f; o1[i] = 0.f; }
#pragma unroll
  for (int s = 0; s < 8; ++s) {
    const bf16x8 pv = lds_ld<bf16x8>(PB + r32 * 272 + 32 * s + 16 * hh);
    o0 = __builtin_amdgcn_mfma_f32_32x32x16_bf16(__builtin_shufflevector(xt[4 * s], xt[4 * s + 1], 0, 1, 2, 3, 4, 5, 6, 7), pv, o0, 0, 0, 0);
    o1 = __builtin_amdgcn_mfma_f32_32x32x16_bf16(__builtin_shufflevector(xt[4 * s + 2], xt[4 * s + 3], 0, 1, 2, 3, 4, 5, 6, 7), pv, o1, 0, 0, 0);
  }
  STAMP(5);
  {
    const unsigned tb = lb + 32768 + w * 4096;
#pragma unroll
    for (int gq = 0; gq < 4; ++gq) {
      lds_st<v2u>(tb + r32 * 128 + 16 * (gq ^ (r32 & 7)) + 8 * hh, v2u{pk_bf(o0[4 * gq], o0[4 * gq + 1]), pk_bf(o0[4 * gq + 2], o0[4 * gq + 3])});
      lds_st<v2u>(tb + r32 * 128 + 16 * ((4 + gq) ^ (r32 & 7)) + 8 * hh, v2u{pk_bf(o1[4 * gq], o1[4 * gq + 1]), pk_bf(o1[4 * gq + 2], o1[4 * gq + 3])});
    }
    asm volatile("s_waitcnt lgkmcnt(0)" ::: "memory");
#pragma unroll
    for (int i = 0; i < 4; ++i) {
      const int head = 8 * i + (lane >> 3);
      const v4u v = lds_ld<v4u>(tb + head * 128 + 16 * ((lane & 7) ^ (head & 7)));
      st16(rs, WS_PART + (((unsigned)a * 32 + head) * 512 + 64 * w + 8 * (lane & 7)) * 2, v);
    }
    dma_slice(p.woA + (size_t)(h * 128) * 512 + 128 * j, 512, lb + 65536, w, lane);
    asm volatile("s_waitcnt vmcnt(4)" ::: "memory");
  }
  wg_barrier();
  if (w == 0 && lane < 32) {
    const f32x4 la = lds_ld<f32x4>(LS + lane * 32), lc = lds_ld<f32x4>(LS + lane * 32 + 16);
    const float l = ((la[0] + la[1]) + (la[2] + la[3])) + ((lc[0] + lc[1]) + (lc[2] + lc[3]));
    st16(rs, WS_ML + ((unsigned)lane * 128 + a) * 16, v4u{g_lo(Mh, tg), tg.hi, g_lo(l, tg), tg.hi});
  }
  STAMP(6);
  const unsigned MLS = scr, R2 = scr + 1024, V16 = scr + 5120;
  if (tid < 128) {
    const v4u v = poll_pair(rs, WS_ML + ((unsigned)h * 128 + tid) * 16, tg);
    lds_st<v2u>(MLS + tid * 8, v2u{v.x << 8, v.z << 8});
  }
  wg_barrier();
  STAMP(7);
  {
    v4u pv[4];
#pragma unroll
    for (int i = 0; i < 4; ++i) pv[i] = ld16(rs, WS_PART + (((unsigned)(16 * w + 4 * i + g) * 32 + h) * 512 + 128 * j + 8 * c16) * 2);
    const int bb = w >> 2;
    const v2u ml = lds_ld<v2u>(MLS + (bb * 64 + lane) * 8);
    const float m = __uint_as_float(ml.x), l = __uint_as_float(ml.y);
    float M = m;
    M = fmaxf(M, dpp<0xB1>(M)); M = fmaxf(M, dpp<0x4E>(M)); M = fmaxf(M, dpp<0x141>(M)); M = fmaxf(M, dpp<0x140>(M)); M = xrow_max(M);
    const float e = __builtin_amdgcn_exp2f((m - M) * LOG2E);
    const float L = xrow_sum(row16_sum(e * l));
    const float wt = e / L;
    float wi[4];
#pragma unroll
    for (int i = 0; i < 4; ++i) wi[i] = __shfl(wt, (16 * w + 4 * i + g) & 63);
    float acc[8];
#pragma unroll
    for (int k = 0; k < 8; ++k) acc[k] = 0.f;
#pragma unroll
    for (int i = 0; i < 4; ++i) {
#pragma unroll
      for (int k = 0; k < 4; ++k) { acc[2 * k] = fmaf(wi[i], bflo(pv[i][k]), acc[2 * k]); acc[2 * k + 1] = fmaf(wi[i], bfhi(pv[i][k]), acc[2 * k + 1]); }
    }
    float r[4], t[2];
#pragma unroll
    for (int k = 0; k < 4; ++k) r[k] = swap32_add(acc[k], acc[k + 4]);
#pragma unroll
    for (int k = 0; k < 2; ++k) t[k] = swap16_add(r[k], r[k + 2]);
    lds_st<v2u>(R2 + (w * 128 + 8 * c16 + 4 * (g >> 1) + 2 * (g & 1)) * 4, v2u{__float_as_uint(t[0]), __float_as_uint(t[1])});
  }
  dma_slice(p.woB + (size_t)(h * 128) * 512 + 128 * j, 512, lb + 98304, w, lane);
  wg_barrier();
  if (tid < 128) {
    const int bb = tid >> 6, cp = tid & 63;
    float x0 = 0.f, x1 = 0.f;
#pragma unroll
    for (int ww = 0; ww < 4; ++ww) { const v2u t = lds_ld<v2u>(R2 + ((4 * bb + ww) * 128 + 2 * cp) * 4); x0 += __uint_as_float(t.x); x1 += __uint_as_float(t.y); }
    lds_st<unsigned>(V16 + tid * 4, pk_bf(x0, x1));
  }
  wg_barrier();
  {
    bf16x8 f[4];
    vec_frags(f, V16, lane);
    const f32x4v d = gemv_mfma(f, lb, w, lane);
    gemv_publish(rs, tg, d, WS_C4 + ((unsigned)h * 4 + j) * 2048, 128, w, lane);
  }
  STAMP(8);
  if (tid < 128) {
    const unsigned v = gather4(rs, tg, WS_C4 + ((unsigned)h * 4) * 2048 + (tid >> 6) * 1024 + (tid & 63) * 16, 2048);
    lds_st<unsigned>(V16 + 512 + tid * 4, v);
  }
  wg_barrier();
  {
    bf16x8 f[4];
    vec_frags(f, V16 + 512, lane);
    const f32x4v d = gemv_mfma(f, lb + 65536, w, lane);
    gemv_publish(rs, tg, d, WS_Y + (((unsigned)0 * 32 + h) * 1024 + 128 * j) * 8, 512, w, lane);
  }
  STAMP(9);
  asm volatile("s_waitcnt vmcnt(0)" ::: "memory");
  if (w == 7) final_reduce(p, rs, tg, 0, a, lane);
  if (tid < 128) {
    const int bb = tid >> 6, dp = tid & 63;
    const unsigned ra = WS_REC + (((unsigned)h * 4 + 2 * bb) * 136) * 8, rb = ra + 136 * 8;
    v4u oa = ld16(rs, ra + dp * 16), ob = ld16(rs, rb + dp * 16), sa = ld16(rs, ra + 1024), sb = ld16(rs, rb + 1024);
    unsigned spins = 0;
    for (;;) {
      const bool ba = !__all(g_ok2(oa, tg)), bo = !__all(g_ok2(ob, tg)), bs = !__all(g_ok2(sa, tg) && g_ok2(sb, tg));
      if (!(ba || bo || bs) || ++spins >= SPIN_CAP) break;
      __builtin_amdgcn_s_sleep(2); asm volatile("" ::: "memory");
      if (ba) oa = ld16(rs, ra + dp * 16);
      if (bo) ob = ld16(rs, rb + dp * 16);
      if (bs) { sa = ld16(rs, ra + 1024); sb = ld16(rs, rb + 1024); }
    }
    const float ma = g_val(sa.z), mb = g_val(sb.z), M = fmaxf(ma, mb);
    const float ea = __builtin_amdgcn_exp2f((ma - M) * LOG2E), eb = __builtin_amdgcn_exp2f((mb - M) * LOG2E);
    const float inv = 1.f / (ea * g_val(sa.x) + eb * g_val(sb.x));
    lds_st<unsigned>(V16 + 1024 + tid * 4, pk_bf((ea * g_val(oa.x) + eb * g_val(ob.x)) * inv, (ea * g_val(oa.z) + eb * g_val(ob.z)) * inv));
  }
  wg_barrier();
  STAMP(10);
  {
    bf16x8 f[4];
    vec_frags(f, V16 + 1024, lane);
    const f32x4v d = gemv_mfma(f, lb + 98304, w, lane);
    gemv_publish(rs, tg, d, WS_Y + (((unsigned)1 * 32 + h) * 1024 + 128 * j) * 8, 512, w, lane);
  }
  STAMP(13);
  if (w == 0) {
    final_reduce(p, rs, tg, 1, a, lane);
    if (a == 0 && lane == 0) __hip_atomic_store(reinterpret_cast<u64*>(p.ws + WS_EPOCH), ((u64)tg.top << 32) | tg.t32, __ATOMIC_RELAXED, __HIP_MEMORY_SCOPE_AGENT);
  }
  STAMP(14);
}

__global__ void __launch_bounds__(512, 2) fused_decode_kernel(Params p) {
  extern __shared__ __attribute__((aligned(16))) char smem[];
  const unsigned lb = (unsigned)(uintptr_t)(LAS char*)smem;
  STAMP(0);
  const rsrc_t rs = __builtin_amdgcn_make_buffer_rsrc(p.ws, 0, WS_END, 0x00020000);
  const u64 E = __hip_atomic_load(reinterpret_cast<u64*>(p.ws + WS_EPOCH), __ATOMIC_RELAXED, __HIP_MEMORY_SCOPE_AGENT);
  const int gidx = blockIdx.x;
  const int role = (gidx >> 3) & 1, k = gidx >> 4, head = 4 * (gidx & 7) + (k >> 2), j = k & 3;
  if (role == 0) stream_wg(p, rs, E, head, j, lb);
  else chain_wg(p, rs, E, head * 4 + j, lb);
}

extern "C" void kernel_launch(void* const* d_in, const int* in_sizes, int n_in, void* d_out, int out_size, void* d_ws, size_t ws_size,
                              hipStream_t stream) {
  Params p;
  p.xA = (const u16*)d_in[0]; p.ckv = (const u16*)d_in[1]; p.wqA = (const u16*)d_in[2]; p.wuk = (const u16*)d_in[3];
  p.wuv = (const u16*)d_in[4]; p.woA = (const u16*)d_in[5]; p.xB = (const u16*)d_in[6]; p.kc = (const u16*)d_in[7];
  p.vc = (const u16*)d_in[8]; p.wqB = (const u16*)d_in[9]; p.woB = (const u16*)d_in[10];
  p.out = (u16*)d_out; p.ws = (unsigned char*)d_ws;
  (void)hipFuncSetAttribute((const void*)fused_decode_kernel, hipFuncAttributeMaxDynamicSharedMemorySize, LDS_BYTES);
  fused_decode_kernel<<<256, 512, LDS_BYTES, stream>>>(p);
}
```

```cpp
#include <hip/hip_runtime.h>
#include <stdint.h>
#define SHPOL " nt"

typedef unsigned short u16;
typedef unsigned long long u64;
typedef unsigned v4u __attribute__((ext_vector_type(4)));
typedef unsigned v2u __attribute__((ext_vector_type(2)));
typedef float f32x4 __attribute__((ext_vector_type(4)));
typedef float f32x16 __attribute__((ext_vector_type(16)));
typedef short bf16x8 __attribute__((ext_vector_type(8)));
typedef short s16x4 __attribute__((ext_vector_type(4)));
typedef __bf16 bf16x2 __attribute__((ext_vector_type(2)));
#define LAS __attribute__((address_space(3)))
#define DI __device__ __forceinline__

constexpr unsigned WS_EPOCH = 0;
constexpr unsigned WS_Q1 = 4096;
constexpr unsigned WS_QL = WS_Q1 + 65536u * 8;
constexpr unsigned WS_ML = WS_QL + 16384u * 8;
constexpr unsigned WS_C4 = WS_ML + 8192u * 8;
constexpr unsigned WS_REC = WS_C4 + 32768u * 8;
constexpr unsigned WS_Y = WS_REC + 32u * 4 * 136 * 8;
constexpr unsigned WS_PART = WS_Y + 65536u * 8;
constexpr unsigned WS_STAMP = WS_PART + 128u * 32 * 512 * 2;
constexpr unsigned WS_END = WS_STAMP + 256u * 16 * 8;

constexpr int LDS_BYTES = 163840;
constexpr unsigned SCR = 131072;
constexpr unsigned SPIN_CAP = 1u << 16;
#define KINIT_ 1
#define KWIN_ 8
#define CK1_ 12
#define VPRE_ 8
#define KSLOW_ 3
#define SWIN_ 0
#define CK0_ 6
constexpr int CK0 = CK0_;
constexpr int KSLOW = KSLOW_, SWIN = SWIN_;
constexpr int KINIT = KINIT_, KWIN = KWIN_, CK1 = CK1_, VPRE = VPRE_;
constexpr float SCALE = 0.08838834764831845f;
constexpr float LOG2E = 1.4426950408889634f;

struct Params {
  const u16 *xA, *ckv, *wqA, *wuk, *wuv, *woA, *xB, *kc, *vc, *wqB, *woB;
  u16* out;
  unsigned char* ws;
};

DI float bflo(unsigned w) { return __uint_as_float(w << 16); }
DI float bfhi(unsigned w) { return __uint_as_float(w & 0xffff0000u); }
DI unsigned f2bf_bits(float f) { return (unsigned)__builtin_bit_cast(unsigned short, (__bf16)f); }
DI float rbf(float f) { return __uint_as_float(f2bf_bits(f) << 16); }
DI unsigned pk_bf(float lo, float hi) { return f2bf_bits(lo) | (f2bf_bits(hi) << 16); }
DI float dot2(unsigned a, unsigned b, float acc) {
  return __builtin_amdgcn_fdot2_f32_bf16(__builtin_bit_cast(bf16x2, a), __builtin_bit_cast(bf16x2, b), acc, false);
}
template <int CTRL> DI float dpp(float x) {
  return __builtin_bit_cast(float, __builtin_amdgcn_mov_dpp(__builtin_bit_cast(int, x), CTRL, 0xf, 0xf, true));
}
DI float row16_sum(float x) { x += dpp<0xB1>(x); x += dpp<0x4E>(x); x += dpp<0x141>(x); x += dpp<0x140>(x); return x; }
DI float xrow_max(float x) {
  auto s = __builtin_amdgcn_permlane16_swap(__float_as_uint(x), __float_as_uint(x), false, false);
  x = fmaxf(__uint_as_float(s[0]), __uint_as_float(s[1]));
  auto t = __builtin_amdgcn_permlane32_swap(__float_as_uint(x), __float_as_uint(x), false, false);
  return fmaxf(__uint_as_float(t[0]), __uint_as_float(t[1]));
}
DI float xrow_sum(float x) {
  auto s = __builtin_amdgcn_permlane16_swap(__float_as_uint(x), __float_as_uint(x), false, false);
  x = __uint_as_float(s[0]) + __uint_as_float(s[1]);
  auto t = __builtin_amdgcn_permlane32_swap(__float_as_uint(x), __float_as_uint(x), false, false);
  return __uint_as_float(t[0]) + __uint_as_float(t[1]);
}
DI float swap32_add(float a, float b) {
  auto t = __builtin_amdgcn_permlane32_swap(__float_as_uint(a), __float_as_uint(b), false, false);
  return __uint_as_float(t[0]) + __uint_as_float(t[1]);
}
DI float swap16_add(float a, float b) {
  auto t = __builtin_amdgcn_permlane16_swap(__float_as_uint(a), __float_as_uint(b), false, false);
  return __uint_as_float(t[0]) + __uint_as_float(t[1]);
}
DI v4u ldnt(const u16* p) { return __builtin_nontemporal_load(reinterpret_cast<const v4u*>(p)); }
DI void wg_barrier() { asm volatile("s_waitcnt lgkmcnt(0)\n\ts_barrier" ::: "memory"); }
DI void glds16_nt(const void* gsrc, unsigned lds_dst) {
  unsigned keep;
  asm volatile("s_mov_b32 %0, m0\n\ts_mov_b32 m0, %2\n\ts_nop 0\n\tglobal_load_lds_dwordx4 %1, off nt\n\ts_mov_b32 m0, %0"
               : "=&s"(keep) : "v"(gsrc), "s"(lds_dst) : "memory");
}
DI void glds16_def(const void* gsrc, unsigned lds_dst) {
  unsigned keep;
  asm volatile("s_mov_b32 %0, m0\n\ts_mov_b32 m0, %2\n\ts_nop 0\n\tglobal_load_lds_dwordx4 %1, off" SHPOL "\n\ts_mov_b32 m0, %0"
               : "=&s"(keep) : "v"(gsrc), "s"(lds_dst) : "memory");
}
typedef __amdgpu_buffer_rsrc_t rsrc_t;
DI rsrc_t mk_rsrc(const void* base, unsigned bytes) {
  const u64 b = (u64)base;
  const unsigned lo = __builtin_amdgcn_readfirstlane((unsigned)b), hi = __builtin_amdgcn_readfirstlane((unsigned)(b >> 32));
  return __builtin_amdgcn_make_buffer_rsrc((void*)(((u64)hi << 32) | lo), 0, bytes, 0x00020000);
}
DI v4u ldnt_b(rsrc_t r, unsigned voff, unsigned soff) { return __builtin_amdgcn_raw_buffer_load_b128(r, voff, soff, 2); }
DI v4u ld16(rsrc_t r, unsigned off) { return __builtin_amdgcn_raw_buffer_load_b128(r, off, 0, 16); }
DI void st8(rsrc_t r, unsigned off, unsigned lo, unsigned hi) { v2u v = {lo, hi}; __builtin_amdgcn_raw_buffer_store_b64(v, r, off, 0, 16); }
DI void st16(rsrc_t r, unsigned off, v4u v) { __builtin_amdgcn_raw_buffer_store_b128(v, r, off, 0, 16); }

struct Tag { unsigned hi, lo8, t32, top; };
DI unsigned g_lo(float v, const Tag& t) { return ((__float_as_uint(v) + 0x80u) >> 8) | (t.lo8 << 24); }
DI float g_val(unsigned lo) { return __uint_as_float(lo << 8); }
DI bool g_ok(unsigned lo, unsigned hi, const Tag& t) { return hi == t.hi && (lo >> 24) == t.lo8; }
DI bool g_ok2(const v4u& v, const Tag& t) { return g_ok(v.x, v.y, t) && g_ok(v.z, v.w, t); }
DI float round24(float v) { return __uint_as_float((__float_as_uint(v) + 0x80u) & 0xffffff00u); }
DI unsigned swz(unsigned r) { return ((r & 3u) << 2) | ((r >> 2) & 3u); }

#define STAMP(k) do { } while (0)

template <typename T> DI void lds_st(unsigned addr, T v) { *(LAS T*)(uintptr_t)addr = v; }
template <typename T> DI T lds_ld(unsigned addr) { return *(LAS T*)(uintptr_t)addr; }

typedef float f32x4v __attribute__((ext_vector_type(4)));

DI void dma_slice(const u16* src0, size_t row_stride, unsigned dst, int w, int lane) {
  asm volatile("" : "+v"(lane));
  const int g = lane >> 4, c16 = lane & 15;
  const u16* s = src0 + (size_t)(16 * w + g) * row_stride;
#pragma unroll
  for (int i = 0; i < 4; ++i) glds16_nt(s + (size_t)(4 * i) * row_stride + 8 * (c16 ^ ((g << 2) | i)), dst + (16 * w + 4 * i) * 256);
}
DI f32x4v gemv_mfma(const bf16x8 (&a)[4], unsigned wbase, int w, int lane) {
  const unsigned g = lane >> 4, q = (lane & 15) >> 2, pp = lane & 3;
  const unsigned chn = 2 * w + (pp >> 1);
  const unsigned a0 = wbase + (8 * g + q) * 256 + 16 * (chn ^ ((q << 2) | ((2 * g) & 3))) + 8 * (pp & 1);
  const unsigned a1 = wbase + (8 * g + 4 + q) * 256 + 16 * (chn ^ ((q << 2) | ((2 * g + 1) & 3))) + 8 * (pp & 1);
  s16x4 x[8];
  asm volatile("ds_read_b64_tr_b16 %0, %8\n\tds_read_b64_tr_b16 %1, %9\n\t"
               "ds_read_b64_tr_b16 %2, %8 offset:8192\n\tds_read_b64_tr_b16 %3, %9 offset:8192\n\t"
               "ds_read_b64_tr_b16 %4, %8 offset:16384\n\tds_read_b64_tr_b16 %5, %9 offset:16384\n\t"
               "ds_read_b64_tr_b16 %6, %8 offset:24576\n\tds_read_b64_tr_b16 %7, %9 offset:24576\n\ts_waitcnt lgkmcnt(0)"
               : "=&v"(x[0]), "=&v"(x[1]), "=&v"(x[2]), "=&v"(x[3]), "=&v"(x[4]), "=&v"(x[5]), "=&v"(x[6]), "=&v"(x[7])
               : "v"(a0), "v"(a1) : "memory");
  f32x4v d = {0.f, 0.f, 0.f, 0.f};
#pragma unroll
  for (int s = 0; s < 4; ++s)
    d = __builtin_amdgcn_mfma_f32_16x16x32_bf16(a[s], __builtin_shufflevector(x[2 * s], x[2 * s + 1], 0, 1, 2, 3, 4, 5, 6, 7), d, 0, 0, 0);
  return d;
}

template <int I0, int I1, bool NT = true> DI void dma_rows1k(const u16* src, unsigned dst, int w, int lane) {
  asm volatile("" : "+v"(lane));
  const u16* s = src + (size_t)(16 * w) * 512;
#pragma unroll
  for (int i = I0; i < I1; ++i) {
    if (NT) glds16_nt(s + i * 512 + 8 * (lane ^ swz(i)), dst + (16 * w + i) * 1024);
    else glds16_def(s + i * 512 + 8 * (lane ^ swz(i)), dst + (16 * w + i) * 1024);
  }
}
DI void vec_frags(bf16x8 (&a)[4], unsigned vbase, int lane) {
  const unsigned ad = vbase + (lane & 1) * 256 + (lane >> 4) * 16;
#pragma unroll
  for (int s = 0; s < 4; ++s) a[s] = lds_ld<bf16x8>(ad + 64 * s);
}
DI void gemv_publish(rsrc_t rs, const Tag& tg, const f32x4v& d, unsigned base, unsigned bstride, int w, int lane) {
  if (lane < 32) {
    const float v = lane < 16 ? d[0] : d[1];
    st8(rs, base + ((lane >> 4) * bstride + 16 * w + (lane & 15)) * 8, g_lo(v, tg), tg.hi);
  }
}
DI unsigned gather4(rsrc_t rs, const Tag& tg, unsigned off, unsigned pstride) {
  v4u v[4];
#pragma unroll
  for (int pj = 0; pj < 4; ++pj) v[pj] = ld16(rs, off + pj * pstride);
  unsigned spins = 0;
  for (;;) {
    bool bad[4], any = false;
#pragma unroll
    for (int pj = 0; pj < 4; ++pj) { bad[pj] = !__all(g_ok2(v[pj], tg)); any = any || bad[pj]; }
    if (!any || ++spins >= SPIN_CAP) break;
    asm volatile("" ::: "memory");
#pragma unroll
    for (int pj = 0; pj < 4; ++pj) if (bad[pj]) v[pj] = ld16(rs, off + pj * pstride);
  }
  return pk_bf((g_val(v[0].x) + g_val(v[1].x)) + (g_val(v[2].x) + g_val(v[3].x)), (g_val(v[0].z) + g_val(v[1].z)) + (g_val(v[2].z) + g_val(v[3].z)));
}
DI Tag make_tag(u64 E) {
  const u64 T = E + 1;
  Tag tg;
  tg.hi = __builtin_amdgcn_readfirstlane((unsigned)(T >> 8));
  tg.lo8 = __builtin_amdgcn_readfirstlane((unsigned)T & 0xffu);
  tg.t32 = __builtin_amdgcn_readfirstlane((unsigned)T);
  tg.top = __builtin_amdgcn_readfirstlane((unsigned)(T >> 32));
  return tg;
}

DI void stream_wg(const Params& p, rsrc_t rs, const u64 E, int h, int j, unsigned lb) {
  const int b = j >> 1, half = j & 1, bh = b * 32 + h;
  const int tid = threadIdx.x, lane = tid & 63, w = __builtin_amdgcn_readfirstlane(tid >> 6), c16 = lane & 15;
  const size_t off = ((size_t)bh * 8192 + 6144 + half * 1024 + w * 128) * 128;
  const rsrc_t rk = mk_rsrc(p.kc + off, 32768), rv = mk_rsrc(p.vc + off, 32768);
  dma_slice(p.wqB + (size_t)(128 * j) * 4096 + h * 128, 4096, lb + 8192, w, lane);
  bf16x8 xa[4];
#pragma unroll
  for (int s = 0; s < 4; ++s) xa[s] = *reinterpret_cast<const bf16x8*>(p.xB + (lane & 1) * 512 + 128 * j + 32 * s + 8 * (lane >> 4));
  v4u K[32], V[32];
#pragma unroll
  for (int i = 0; i < KINIT; ++i) K[i] = ldnt_b(rk, lane * 16, i * 1024);
  asm volatile("s_waitcnt vmcnt(%0)" :: "i"(KINIT) : "memory");
  wg_barrier();
  const Tag tg = make_tag(E);
  {
    const f32x4v d = gemv_mfma(xa, lb + 8192, w, lane);
    gemv_publish(rs, tg, d, WS_Q1 + (((unsigned)h * 4 + j) * 2 + 1) * 2048, 128, w, lane);
  }
  STAMP(1);
  v4u qa[4];
  const unsigned qoff = WS_Q1 + ((((unsigned)h * 4) * 2 + 1) * 2 + b) * 1024 + lane * 16;
#pragma unroll
  for (int i = KINIT; i < 32 + VPRE; ++i) {
    if (i < 32) K[i] = ldnt_b(rk, lane * 16, i * 1024); else V[i - 32] = ldnt_b(rv, lane * 16, (i - 32) * 1024);
    if (i == 24) { if (w == 0) {
#pragma unroll
        for (int pj = 0; pj < 4; ++pj) qa[pj] = ld16(rs, qoff + pj * 4096); } }
    if (i < KINIT + KSLOW) asm volatile("s_waitcnt vmcnt(%0)" :: "i"(SWIN) : "memory");
    else if (i >= KWIN) asm volatile("s_waitcnt vmcnt(%0)" :: "i"(KWIN) : "memory");
  }
  if (w == 0) {
    unsigned spins = 0;
    while (!__all(g_ok2(qa[0], tg) && g_ok2(qa[1], tg) && g_ok2(qa[2], tg) && g_ok2(qa[3], tg)) && ++spins < SPIN_CAP) {
      asm volatile("" ::: "memory");
#pragma unroll
      for (int pj = 0; pj < 4; ++pj) qa[pj] = ld16(rs, qoff + pj * 4096);
    }
    const float q0 = (g_val(qa[0].x) + g_val(qa[1].x)) + (g_val(qa[2].x) + g_val(qa[3].x));
    const float q1 = (g_val(qa[0].z) + g_val(qa[1].z)) + (g_val(qa[2].z) + g_val(qa[3].z));
    lds_st<unsigned>(lb + lane * 4, pk_bf(q0, q1));
  }
  wg_barrier();
  STAMP(2);
  const v4u q = lds_ld<v4u>(lb + c16 * 16);
  float s[32];
#pragma unroll
  for (int i = 0; i < 32; ++i) {
    float d = dot2(K[i].x, q.x, 0.f); d = dot2(K[i].y, q.y, d); d = dot2(K[i].z, q.z, d); d = dot2(K[i].w, q.w, d);
    if (i + VPRE < 32) V[i + VPRE] = ldnt_b(rv, lane * 16, (i + VPRE) * 1024);
    s[i] = d;
  }
  float m = -INFINITY;
#pragma unroll
  for (int i = 0; i < 32; ++i) { s[i] = rbf(rbf(row16_sum(s[i])) * SCALE); m = fmaxf(m, s[i]); }
  m = xrow_max(m);
  float l = 0.f, acc[8];
#pragma unroll
  for (int k = 0; k < 8; ++k) acc[k] = 0.f;
  const float mneg = -m * LOG2E;
#pragma unroll
  for (int i = 0; i < 32; ++i) { s[i] = __builtin_amdgcn_exp2f(fmaf(s[i], LOG2E, mneg)); l += s[i]; }
  l = xrow_sum(l);
  const unsigned ST = lb + 512, mg = lb + 1024;
  if (lane == 0) lds_st<v2u>(ST + w * 8, v2u{__float_as_uint(m), __float_as_uint(l)});
  wg_barrier();
  float M, L;
  {
    const f32x4 s0 = lds_ld<f32x4>(ST), s1 = lds_ld<f32x4>(ST + 16), s2 = lds_ld<f32x4>(ST + 32), s3 = lds_ld<f32x4>(ST + 48);
    M = fmaxf(fmaxf(fmaxf(s0[0], s0[2]), fmaxf(s1[0], s1[2])), fmaxf(fmaxf(s2[0], s2[2]), fmaxf(s3[0], s3[2])));
    L = __builtin_amdgcn_exp2f((s0[0] - M) * LOG2E) * s0[1] + __builtin_amdgcn_exp2f((s0[2] - M) * LOG2E) * s0[3]
      + __builtin_amdgcn_exp2f((s1[0] - M) * LOG2E) * s1[1] + __builtin_amdgcn_exp2f((s1[2] - M) * LOG2E) * s1[3]
      + __builtin_amdgcn_exp2f((s2[0] - M) * LOG2E) * s2[1] + __builtin_amdgcn_exp2f((s2[2] - M) * LOG2E) * s2[3]
      + __builtin_amdgcn_exp2f((s3[0] - M) * LOG2E) * s3[1] + __builtin_amdgcn_exp2f((s3[2] - M) * LOG2E) * s3[3];
  }
  const float ew = __builtin_amdgcn_exp2f((m - M) * LOG2E);
  if (tid == 0) st16(rs, WS_REC + (((unsigned)h * 4 + j) * 136 + 128) * 8, v4u{g_lo(L, tg), tg.hi, g_lo(M, tg), tg.hi});
  STAMP(3);
#pragma unroll
  for (int i = 0; i < 32; ++i) {
#pragma unroll
    for (int k = 0; k < 4; ++k) { acc[2 * k] = fmaf(s[i], bflo(V[i][k]), acc[2 * k]); acc[2 * k + 1] = fmaf(s[i], bfhi(V[i][k]), acc[2 * k + 1]); }
  }
  float r[4], t[2];
#pragma unroll
  for (int k = 0; k < 4; ++k) r[k] = swap32_add(acc[k], acc[k + 4]);
#pragma unroll
  for (int k = 0; k < 2; ++k) t[k] = swap16_add(r[k], r[k + 2]) * ew;
  {
    const int g = lane >> 4;
    lds_st<v2u>(mg + w * 512 + (8 * c16 + 4 * (g >> 1) + 2 * (g & 1)) * 4, v2u{__float_as_uint(t[0]), __float_as_uint(t[1])});
  }
  wg_barrier();
  STAMP(4);
  if (tid < 128) {
    float o = 0.f;
#pragma unroll
    for (int ww = 0; ww < 8; ++ww) o += lds_ld<float>(mg + ww * 512 + tid * 4);
    st8(rs, WS_REC + (((unsigned)h * 4 + j) * 136 + tid) * 8, g_lo(o, tg), tg.hi);
  }
  STAMP(5);
}

DI v4u poll_pair(rsrc_t rs, unsigned off, const Tag& tg) {
  v4u v = ld16(rs, off);
  unsigned spins = 0;
  while (!__all(g_ok2(v, tg)) && ++spins < SPIN_CAP) { __builtin_amdgcn_s_sleep(1); asm volatile("" ::: "memory"); v = ld16(rs, off); }
  return v;
}

DI void final_reduce(const Params& p, rsrc_t rs, const Tag& tg, int which, int a, int lane, u64 t0 = 0) {
  const int b = a >> 6, n0 = (a & 63) * 8, head = lane >> 1, hf = lane & 1;
  const unsigned off = WS_Y + ((((unsigned)which * 32 + head) * 2 + b) * 512 + n0 + 4 * hf) * 8;
  v4u v0 = ld16(rs, off), v1 = ld16(rs, off + 16);
  unsigned spins = 0;
  while (!(g_ok2(v0, tg) && g_ok2(v1, tg)) && ++spins < SPIN_CAP) { __builtin_amdgcn_s_sleep(1); asm volatile("" ::: "memory"); v0 = ld16(rs, off); v1 = ld16(rs, off + 16); }
  float r0 = swap32_add(g_val(v0.x), g_val(v1.x)), r1 = swap32_add(g_val(v0.z), g_val(v1.z));
  float x = swap16_add(r0, r1);
  x += dpp<0x4E>(x);
  x += dpp<0x124>(x);
  x += dpp<0x128>(x);
  if (t0 && a == 0 && lane == 0) x += (float)(__builtin_amdgcn_s_memrealtime() - t0) * 0.01f * 4e-6f;
  if ((lane & 15) < 2) p.out[b * 1024 + which * 512 + n0 + 4 * hf + (lane >> 4)] = (u16)f2bf_bits(x);
}

DI void chain_wg(const Params& p, rsrc_t rs, const u64 E, int a, unsigned lb) {
  const u64 tstart = __builtin_amdgcn_s_memrealtime();
  const int h = a >> 2, j = a & 3;
  const int tid = threadIdx.x, lane = tid & 63, w = __builtin_amdgcn_readfirstlane(tid >> 6), g = lane >> 4, c16 = lane & 15;
  const unsigned scr = lb + SCR;
  dma_slice(p.wqA + (size_t)(128 * j) * 4096 + h * 128, 4096, scr, w, lane);
  bf16x8 fa[4], WK[4];
#pragma unroll
  for (int s = 0; s < 4; ++s) fa[s] = *reinterpret_cast<const bf16x8*>(p.xA + (lane & 1) * 512 + 128 * j + 32 * s + 8 * g);
  {
    const rsrc_t rw = mk_rsrc(p.wuk + (size_t)(128 * j + 16 * w) * 4096 + h * 128, 16 * 8192);
#pragma unroll
    for (int s = 0; s < 4; ++s) WK[s] = __builtin_bit_cast(bf16x8, ldnt_b(rw, c16 * 8192 + g * 16, 64 * s));
  }
  const int bA = a >> 6;
  asm volatile("s_barrier" ::: "memory");
  dma_rows1k<0, CK0>(p.ckv + (size_t)a * 128 * 512, lb, w, lane);
  asm volatile("s_waitcnt vmcnt(%0)" :: "i"(4 + CK0) : "memory");
  wg_barrier();
  const Tag tg = make_tag(E);
  {
    const f32x4v d = gemv_mfma(fa, scr, w, lane);
    gemv_publish(rs, tg, d, WS_Q1 + (((unsigned)h * 4 + j) * 2 + 0) * 2048, 128, w, lane);
  }
  STAMP(1);
  dma_rows1k<CK0, CK1>(p.ckv + (size_t)a * 128 * 512, lb, w, lane);
  wg_barrier();
  if (tid < 128) {
    const unsigned v = gather4(rs, tg, WS_Q1 + (((unsigned)h * 4) * 4 + (tid >> 6)) * 1024 + (tid & 63) * 16, 4096);
    lds_st<unsigned>(scr + tid * 4, v);
  }
  wg_barrier();
  STAMP(2);
  {
    bf16x8 qf[4];
    vec_frags(qf, scr, lane);
    f32x4v d = {0.f, 0.f, 0.f, 0.f};
#pragma unroll
    for (int s = 0; s < 4; ++s) d = __builtin_amdgcn_mfma_f32_16x16x32_bf16(WK[s], qf[s], d, 0, 0, 0);
    if (c16 < 2)
      st16(rs, WS_QL + (((unsigned)c16 * 32 + h) * 256 + 64 * j + 8 * w + 2 * g) * 8, v4u{pk_bf(d[0], d[1]), tg.t32, pk_bf(d[2], d[3]), tg.t32});
  }
  STAMP(3);
  dma_rows1k<CK1, 16>(p.ckv + (size_t)a * 128 * 512, lb, w, lane);
  const int r32 = lane & 31, hh = lane >> 5;
  float sc[8];
  s16x4 xt[32];
  bf16x8 ka[16];
  const unsigned fr = swz(r32);
  {
    v4u v[8];
    const unsigned base = WS_QL + (unsigned)bA * 65536 + tid * 16;
#pragma unroll
    for (int i = 0; i < 8; ++i) { v[i] = ld16(rs, base + i * 8192); }
    asm volatile("s_waitcnt vmcnt(8)" ::: "memory");
    wg_barrier();
    STAMP(11);
    {
      const unsigned abase = lb + (32 * (w & 3) + r32) * 1024 + 512 * (w >> 2);
#pragma unroll
      for (int s = 0; s < 16; ++s) ka[s] = lds_ld<bf16x8>(abase + 16 * ((unsigned)((2 * s + hh) & 15) ^ fr) + 256 * (s >> 3));
      const unsigned blk = (lane >> 4) & 1, q = (lane & 15) >> 2, pp = lane & 3;
      unsigned tb[4];
#pragma unroll
      for (int mt = 0; mt < 2; ++mt)
#pragma unroll
        for (int t = 0; t < 2; ++t) {
          const unsigned row = 8 * hh + 4 * t + q, chn = 8 * w + 4 * mt + 2 * blk + (pp >> 1);
          tb[mt * 2 + t] = lb + row * 1024 + 16 * (chn ^ swz(row)) + 8 * (pp & 1);
        }
#pragma unroll
      for (int hf = 0; hf < 2; ++hf) {
        asm volatile(
            "ds_read_b64_tr_b16 %0, %16\n\tds_read_b64_tr_b16 %1, %17\n\tds_read_b64_tr_b16 %2, %18\n\tds_read_b64_tr_b16 %3, %19\n\t"
            "ds_read_b64_tr_b16 %4, %16 offset:16384\n\tds_read_b64_tr_b16 %5, %17 offset:16384\n\tds_read_b64_tr_b16 %6, %18 offset:16384\n\tds_read_b64_tr_b16 %7, %19 offset:16384\n\t"
            "ds_read_b64_tr_b16 %8, %16 offset:32768\n\tds_read_b64_tr_b16 %9, %17 offset:32768\n\tds_read_b64_tr_b16 %10, %18 offset:32768\n\tds_read_b64_tr_b16 %11, %19 offset:32768\n\t"
            "ds_read_b64_tr_b16 %12, %16 offset:49152\n\tds_read_b64_tr_b16 %13, %17 offset:49152\n\tds_read_b64_tr_b16 %14, %18 offset:49152\n\tds_read_b64_tr_b16 %15, %19 offset:49152\n\t"
            "s_waitcnt lgkmcnt(0)"
            : "=&v"(xt[16 * hf + 0]), "=&v"(xt[16 * hf + 1]), "=&v"(xt[16 * hf + 2]), "=&v"(xt[16 * hf + 3]),
              "=&v"(xt[16 * hf + 4]), "=&v"(xt[16 * hf + 5]), "=&v"(xt[16 * hf + 6]), "=&v"(xt[16 * hf + 7]),
              "=&v"(xt[16 * hf + 8]), "=&v"(xt[16 * hf + 9]), "=&v"(xt[16 * hf + 10]), "=&v"(xt[16 * hf + 11]),
              "=&v"(xt[16 * hf + 12]), "=&v"(xt[16 * hf + 13]), "=&v"(xt[16 * hf + 14]), "=&v"(xt[16 * hf + 15])
            : "v"(tb[0] + hf * 65536), "v"(tb[1] + hf * 65536), "v"(tb[2] + hf * 65536), "v"(tb[3] + hf * 65536) : "memory");
      }
    }
    STAMP(12);
    unsigned spins = 0;
    const unsigned cq = tid & 127, h0 = tid >> 7;
    for (;;) {
      bool bad[8], any = false;
#pragma unroll
      for (int i = 0; i < 8; ++i) { bad[i] = !__all(v[i].y == tg.t32 && v[i].w == tg.t32); any = any || bad[i]; }
      if (!any || ++spins >= SPIN_CAP) break;
      asm volatile("" ::: "memory");
#pragma unroll
      for (int i = 0; i < 8; ++i) if (bad[i]) v[i] = ld16(rs, base + i * 8192);
    }
#pragma unroll
    for (int i = 0; i < 8; ++i) {
      const unsigned head = 4 * i + h0;
      lds_st<v2u>(scr + head * 1024 + 16 * ((cq >> 1) ^ swz(head)) + 8 * (cq & 1), v2u{v[i].x, v[i].z});
    }
  }
  wg_barrier();
  STAMP(4);
  dma_slice(p.wuv + (size_t)(128 * j) * 4096 + h * 128, 4096, lb, w, lane);
  {
    const int ch = w >> 2;
    f32x16 acc;
#pragma unroll
    for (int i = 0; i < 16; ++i) acc[i] = 0.f;
    const unsigned bbase = scr + r32 * 1024 + 512 * ch;
#pragma unroll
    for (int s = 0; s < 16; ++s) {
      const bf16x8 bv = lds_ld<bf16x8>(bbase + 16 * ((unsigned)((2 * s + hh) & 15) ^ fr) + 256 * (s >> 3));
      acc = __builtin_amdgcn_mfma_f32_32x32x16_bf16(ka[s], bv, acc, 0, 0, 0);
      if ((s & 7) == 7) __builtin_amdgcn_sched_barrier(0);
    }
    f32x4 s0, s1;
#pragma unroll
    for (int k = 0; k < 4; ++k) { s0[k] = ch ? acc[k] : acc[8 + k]; s1[k] = ch ? acc[4 + k] : acc[12 + k]; }
    const unsigned XB = lb + 32768;
    lds_st<f32x4>(XB + w * 2048 + lane * 16, s0);
    lds_st<f32x4>(XB + w * 2048 + 1024 + lane * 16, s1);
    wg_barrier();
    const f32x4 t0 = lds_ld<f32x4>(XB + (w ^ 4) * 2048 + lane * 16), t1 = lds_ld<f32x4>(XB + (w ^ 4) * 2048 + 1024 + lane * 16);
#pragma unroll
    for (int k = 0; k < 4; ++k) { sc[k] = ((ch ? acc[8 + k] : acc[k]) + t0[k]) * SCALE; sc[4 + k] = ((ch ? acc[12 + k] : acc[4 + k]) + t1[k]) * SCALE; }
  }
  const unsigned MX = scr + 16384, LS = scr + 17408, PB = scr + 18432;
  float Mh;
  {
    float m = sc[0];
#pragma unroll
    for (int k = 1; k < 8; ++k) m = fmaxf(m, sc[k]);
    { auto t = __builtin_amdgcn_permlane32_swap(__float_as_uint(m), __float_as_uint(m), false, false); m = fmaxf(__uint_as_float(t[0]), __uint_as_float(t[1])); }
    if (lane < 32) lds_st<float>(MX + (lane * 8 + w) * 4, m);
    wg_barrier();
    const f32x4 ma = lds_ld<f32x4>(MX + r32 * 32), mb = lds_ld<f32x4>(MX + r32 * 32 + 16);
    float M = fmaxf(fmaxf(fmaxf(ma[0], ma[1]), fmaxf(ma[2], ma[3])), fmaxf(fmaxf(mb[0], mb[1]), fmaxf(mb[2], mb[3])));
    M = round24(M);
    Mh = M;
    const float mneg = -M * LOG2E;
    unsigned pk[4]; float l = 0.f;
#pragma unroll
    for (int k = 0; k < 4; ++k) {
      const float p0 = __builtin_amdgcn_exp2f(fmaf(sc[2 * k], LOG2E, mneg)), p1 = __builtin_amdgcn_exp2f(fmaf(sc[2 * k + 1], LOG2E, mneg));
      pk[k] = pk_bf(p0, p1);
      l += bflo(pk[k]) + bfhi(pk[k]);
    }
    { auto t = __builtin_amdgcn_permlane32_swap(__float_as_uint(l), __float_as_uint(l), false, false); l = __uint_as_float(t[0]) + __uint_as_float(t[1]); }
    if (lane < 32) lds_st<float>(LS + (lane * 8 + w) * 4, l);
    const int kt = w & 3, ch = w >> 2;
    lds_st<v2u>(PB + r32 * 272 + (32 * kt + 16 * ch + 4 * hh) * 2, v2u{pk[0], pk[1]});
    lds_st<v2u>(PB + r32 * 272 + (32 * kt + 16 * ch + 8 + 4 * hh) * 2, v2u{pk[2], pk[3]});
  }
  wg_barrier();
  f32x16 o0, o1;
#pragma unroll
  for (int i = 0; i < 16; ++i) { o0[i] = 0.f; o1[i] = 0.f; }
#pragma unroll
  for (int s = 0; s < 8; ++s) {
    const bf16x8 pv = lds_ld<bf16x8>(PB + r32 * 272 + 32 * s + 16 * hh);
    o0 = __builtin_amdgcn_mfma_f32_32x32x16_bf16(__builtin_shufflevector(xt[4 * s], xt[4 * s + 1], 0, 1, 2, 3, 4, 5, 6, 7), pv, o0, 0, 0, 0);
    o1 = __builtin_amdgcn_mfma_f32_32x32x16_bf16(__builtin_shufflevector(xt[4 * s + 2], xt[4 * s + 3], 0, 1, 2, 3, 4, 5, 6, 7), pv, o1, 0, 0, 0);
  }
  STAMP(5);
  {
    const unsigned tb = lb + 32768 + w * 4096;
#pragma unroll
    for (int gq = 0; gq < 4; ++gq) {
      lds_st<v2u>(tb + r32 * 128 + 16 * (gq ^ (r32 & 7)) + 8 * hh, v2u{pk_bf(o0[4 * gq], o0[4 * gq + 1]), pk_bf(o0[4 * gq + 2], o0[4 * gq + 3])});
      lds_st<v2u>(tb + r32 * 128 + 16 * ((4 + gq) ^ (r32 & 7)) + 8 * hh, v2u{pk_bf(o1[4 * gq], o1[4 * gq + 1]), pk_bf(o1[4 * gq + 2], o1[4 * gq + 3])});
    }
    asm volatile("s_waitcnt lgkmcnt(0)" ::: "memory");
#pragma unroll
    for (int i = 0; i < 4; ++i) {
      const int head = 8 * i + (lane >> 3);
      const v4u v = lds_ld<v4u>(tb + head * 128 + 16 * ((lane & 7) ^ (head & 7)));
      st16(rs, WS_PART + (((unsigned)a * 32 + head) * 512 + 64 * w + 8 * (lane & 7)) * 2, v);
    }
    dma_slice(p.woA + (size_t)(h * 128) * 512 + 128 * j, 512, lb + 65536, w, lane);
    asm volatile("s_waitcnt vmcnt(4)" ::: "memory");
  }
  wg_barrier();
  if (w == 0 && lane < 32) {
    const f32x4 la = lds_ld<f32x4>(LS + lane * 32), lc = lds_ld<f32x4>(LS + lane * 32 + 16);
    const float l = ((la[0] + la[1]) + (la[2] + la[3])) + ((lc[0] + lc[1]) + (lc[2] + lc[3]));
    st16(rs, WS_ML + ((unsigned)lane * 128 + a) * 16, v4u{g_lo(Mh, tg), tg.hi, g_lo(l, tg), tg.hi});
  }
  STAMP(6);
  const unsigned MLS = scr, R2 = scr + 1024, V16 = scr + 5120;
  if (tid < 128) {
    const v4u v = poll_pair(rs, WS_ML + ((unsigned)h * 128 + tid) * 16, tg);
    lds_st<v2u>(MLS + tid * 8, v2u{v.x << 8, v.z << 8});
  }
  wg_barrier();
  STAMP(7);
  {
    v4u pv[4];
#pragma unroll
    for (int i = 0; i < 4; ++i) pv[i] = ld16(rs, WS_PART + (((unsigned)(16 * w + 4 * i + g) * 32 + h) * 512 + 128 * j + 8 * c16) * 2);
    const int bb = w >> 2;
    const v2u ml = lds_ld<v2u>(MLS + (bb * 64 + lane) * 8);
    const float m = __uint_as_float(ml.x), l = __uint_as_float(ml.y);
    float M = m;
    M = fmaxf(M, dpp<0xB1>(M)); M = fmaxf(M, dpp<0x4E>(M)); M = fmaxf(M, dpp<0x141>(M)); M = fmaxf(M, dpp<0x140>(M)); M = xrow_max(M);
    const float e = __builtin_amdgcn_exp2f((m - M) * LOG2E);
    const float L = xrow_sum(row16_sum(e * l));
    const float wt = e / L;
    float wi[4];
#pragma unroll
    for (int i = 0; i < 4; ++i) wi[i] = __shfl(wt, (16 * w + 4 * i + g) & 63);
    float acc[8];
#pragma unroll
    for (int k = 0; k < 8; ++k) acc[k] = 0.f;
#pragma unroll
    for (int i = 0; i < 4; ++i) {
#pragma unroll
      for (int k = 0; k < 4; ++k) { acc[2 * k] = fmaf(wi[i], bflo(pv[i][k]), acc[2 * k]); acc[2 * k + 1] = fmaf(wi[i], bfhi(pv[i][k]), acc[2 * k + 1]); }
    }
    float r[4], t[2];
#pragma unroll
    for (int k = 0; k < 4; ++k) r[k] = swap32_add(acc[k], acc[k + 4]);
#pragma unroll
    for (int k = 0; k < 2; ++k) t[k] = swap16_add(r[k], r[k + 2]);
    lds_st<v2u>(R2 + (w * 128 + 8 * c16 + 4 * (g >> 1) + 2 * (g & 1)) * 4, v2u{__float_as_uint(t[0]), __float_as_uint(t[1])});
  }
  dma_slice(p.woB + (size_t)(h * 128) * 512 + 128 * j, 512, lb + 98304, w, lane);
  wg_barrier();
  if (tid < 128) {
    const int bb = tid >> 6, cp = tid & 63;
    float x0 = 0.f, x1 = 0.f;
#pragma unroll
    for (int ww = 0; ww < 4; ++ww) { const v2u t = lds_ld<v2u>(R2 + ((4 * bb + ww) * 128 + 2 * cp) * 4); x0 += __uint_as_float(t.x); x1 += __uint_as_float(t.y); }
    lds_st<unsigned>(V16 + tid * 4, pk_bf(x0, x1));
  }
  wg_barrier();
  {
    bf16x8 f[4];
    vec_frags(f, V16, lane);
    const f32x4v d = gemv_mfma(f, lb, w, lane);
    gemv_publish(rs, tg, d, WS_C4 + ((unsigned)h * 4 + j) * 2048, 128, w, lane);
  }
  STAMP(8);
  if (tid < 128) {
    const unsigned v = gather4(rs, tg, WS_C4 + ((unsigned)h * 4) * 2048 + (tid >> 6) * 1024 + (tid & 63) * 16, 2048);
    lds_st<unsigned>(V16 + 512 + tid * 4, v);
  }
  wg_barrier();
  {
    bf16x8 f[4];
    vec_frags(f, V16 + 512, lane);
    const f32x4v d = gemv_mfma(f, lb + 65536, w, lane);
    gemv_publish(rs, tg, d, WS_Y + (((unsigned)0 * 32 + h) * 1024 + 128 * j) * 8, 512, w, lane);
  }
  STAMP(9);
  asm volatile("s_waitcnt vmcnt(0)" ::: "memory");
  if (w == 7) final_reduce(p, rs, tg, 0, a, lane);
  if (tid < 128) {
    const int bb = tid >> 6, dp = tid & 63;
    const unsigned ra = WS_REC + (((unsigned)h * 4 + 2 * bb) * 136) * 8, rb = ra + 136 * 8;
    v4u oa = ld16(rs, ra + dp * 16), ob = ld16(rs, rb + dp * 16), sa = ld16(rs, ra + 1024), sb = ld16(rs, rb + 1024);
    unsigned spins = 0;
    for (;;) {
      const bool ba = !__all(g_ok2(oa, tg)), bo = !__all(g_ok2(ob, tg)), bs = !__all(g_ok2(sa, tg) && g_ok2(sb, tg));
      if (!(ba || bo || bs) || ++spins >= SPIN_CAP) break;
      __builtin_amdgcn_s_sleep(2); asm volatile("" ::: "memory");
      if (ba) oa = ld16(rs, ra + dp * 16);
      if (bo) ob = ld16(rs, rb + dp * 16);
      if (bs) { sa = ld16(rs, ra + 1024); sb = ld16(rs, rb + 1024); }
    }
    const float ma = g_val(sa.z), mb = g_val(sb.z), M = fmaxf(ma, mb);
    const float ea = __builtin_amdgcn_exp2f((ma - M) * LOG2E), eb = __builtin_amdgcn_exp2f((mb - M) * LOG2E);
    const float inv = 1.f / (ea * g_val(sa.x) + eb * g_val(sb.x));
    lds_st<unsigned>(V16 + 1024 + tid * 4, pk_bf((ea * g_val(oa.x) + eb * g_val(ob.x)) * inv, (ea * g_val(oa.z) + eb * g_val(ob.z)) * inv));
  }
  wg_barrier();
  STAMP(10);
  {
    bf16x8 f[4];
    vec_frags(f, V16 + 1024, lane);
    const f32x4v d = gemv_mfma(f, lb + 98304, w, lane);
    gemv_publish(rs, tg, d, WS_Y + (((unsigned)1 * 32 + h) * 1024 + 128 * j) * 8, 512, w, lane);
  }
  STAMP(13);
  if (w == 0) {
    final_reduce(p, rs, tg, 1, a, lane, tstart);
    if (a == 0 && lane == 0) __hip_atomic_store(reinterpret_cast<u64*>(p.ws + WS_EPOCH), ((u64)tg.top << 32) | tg.t32, __ATOMIC_RELAXED, __HIP_MEMORY_SCOPE_AGENT);
  }
  STAMP(14);
}

__global__ void __launch_bounds__(512, 2) fused_decode_kernel(Params p) {
  extern __shared__ __attribute__((aligned(16))) char smem[];
  const unsigned lb = (unsigned)(uintptr_t)(LAS char*)smem;
  STAMP(0);
  const rsrc_t rs = __builtin_amdgcn_make_buffer_rsrc(p.ws, 0, WS_END, 0x00020000);
  const u64 E = __hip_atomic_load(reinterpret_cast<u64*>(p.ws + WS_EPOCH), __ATOMIC_RELAXED, __HIP_MEMORY_SCOPE_AGENT);
  const int gidx = blockIdx.x;
  const int role = (gidx >> 3) & 1, k = gidx >> 4, head = 4 * (gidx & 7) + (k >> 2), j = k & 3;
  if (role == 0) stream_wg(p, rs, E, head, j, lb);
  else chain_wg(p, rs, E, head * 4 + j, lb);
}

extern "C" void kernel_launch(void* const* d_in, const int* in_sizes, int n_in, void* d_out, int out_size, void* d_ws, size_t ws_size,
                              hipStream_t stream) {
  Params p;
  p.xA = (const u16*)d_in[0]; p.ckv = (const u16*)d_in[1]; p.wqA = (const u16*)d_in[2]; p.wuk = (const u16*)d_in[3];
  p.wuv = (const u16*)d_in[4]; p.woA = (const u16*)d_in[5]; p.xB = (const u16*)d_in[6]; p.kc = (const u16*)d_in[7];
  p.vc = (const u16*)d_in[8]; p.wqB = (const u16*)d_in[9]; p.woB = (const u16*)d_in[10];
  p.out = (u16*)d_out; p.ws = (unsigned char*)d_ws;
  (void)hipFuncSetAttribute((const void*)fused_decode_kernel, hipFuncAttributeMaxDynamicSharedMemorySize, LDS_BYTES);
  fused_decode_kernel<<<256, 512, LDS_BYTES, stream>>>(p);
}
```

```cpp
#include <hip/hip_runtime.h>
#include <stdint.h>
#define SHPOL " nt"

typedef unsigned short u16;
typedef unsigned long long u64;
typedef unsigned v4u __attribute__((ext_vector_type(4)));
typedef unsigned v2u __attribute__((ext_vector_type(2)));
typedef float f32x4 __attribute__((ext_vector_type(4)));
typedef float f32x16 __attribute__((ext_vector_type(16)));
typedef short bf16x8 __attribute__((ext_vector_type(8)));
typedef short s16x4 __attribute__((ext_vector_type(4)));
typedef __bf16 bf16x2 __attribute__((ext_vector_type(2)));
#define LAS __attribute__((address_space(3)))
#define DI __device__ __forceinline__

constexpr unsigned WS_EPOCH = 0;
constexpr unsigned WS_Q1 = 4096;
constexpr unsigned WS_QL = WS_Q1 + 65536u * 8;
constexpr unsigned WS_ML = WS_QL + 16384u * 8;
constexpr unsigned WS_C4 = WS_ML + 8192u * 8;
constexpr unsigned WS_REC = WS_C4 + 32768u * 8;
constexpr unsigned WS_Y = WS_REC + 32u * 4 * 136 * 8;
constexpr unsigned WS_PART = WS_Y + 65536u * 8;
constexpr unsigned WS_STAMP = WS_PART + 128u * 32 * 512 * 2;
constexpr unsigned WS_END = WS_STAMP + 256u * 16 * 8;

constexpr unsigned CODE_BYTES = 23552;
constexpr int LDS_BYTES = 163840;
constexpr unsigned SCR = 131072;
constexpr unsigned SPIN_CAP = 1u << 16;
#define KINIT_ 1
#define KWIN_ 8
#define CK1_ 12
#define VPRE_ 8
#define KSLOW_ 3
#define SWIN_ 0
#define CK0_ 6
constexpr int CK0 = CK0_;
constexpr int KSLOW = KSLOW_, SWIN = SWIN_;
constexpr int KINIT = KINIT_, KWIN = KWIN_, CK1 = CK1_, VPRE = VPRE_;
constexpr float SCALE = 0.08838834764831845f;
constexpr float LOG2E = 1.4426950408889634f;

struct Params {
  const u16 *xA, *ckv, *wqA, *wuk, *wuv, *woA, *xB, *kc, *vc, *wqB, *woB;
  u16* out;
  unsigned char* ws;
};

DI float bflo(unsigned w) { return __uint_as_float(w << 16); }
DI float bfhi(unsigned w) { return __uint_as_float(w & 0xffff0000u); }
DI unsigned f2bf_bits(float f) { return (unsigned)__builtin_bit_cast(unsigned short, (__bf16)f); }
DI float rbf(float f) { return __uint_as_float(f2bf_bits(f) << 16); }
DI unsigned pk_bf(float lo, float hi) { return f2bf_bits(lo) | (f2bf_bits(hi) << 16); }
DI float dot2(unsigned a, unsigned b, float acc) {
  return __builtin_amdgcn_fdot2_f32_bf16(__builtin_bit_cast(bf16x2, a), __builtin_bit_cast(bf16x2, b), acc, false);
}
template <int CTRL> DI float dpp(float x) {
  return __builtin_bit_cast(float, __builtin_amdgcn_mov_dpp(__builtin_bit_cast(int, x), CTRL, 0xf, 0xf, true));
}
DI float row16_sum(float x) { x += dpp<0xB1>(x); x += dpp<0x4E>(x); x += dpp<0x141>(x); x += dpp<0x140>(x); return x; }
DI float xrow_max(float x) {
  auto s = __builtin_amdgcn_permlane16_swap(__float_as_uint(x), __float_as_uint(x), false, false);
  x = fmaxf(__uint_as_float(s[0]), __uint_as_float(s[1]));
  auto t = __builtin_amdgcn_permlane32_swap(__float_as_uint(x), __float_as_uint(x), false, false);
  return fmaxf(__uint_as_float(t[0]), __uint_as_float(t[1]));
}
DI float xrow_sum(float x) {
  auto s = __builtin_amdgcn_permlane16_swap(__float_as_uint(x), __float_as_uint(x), false, false);
  x = __uint_as_float(s[0]) + __uint_as_float(s[1]);
  auto t = __builtin_amdgcn_permlane32_swap(__float_as_uint(x), __float_as_uint(x), false, false);
  return __uint_as_float(t[0]) + __uint_as_float(t[1]);
}
DI float swap32_add(float a, float b) {
  auto t = __builtin_amdgcn_permlane32_swap(__float_as_uint(a), __float_as_uint(b), false, false);
  return __uint_as_float(t[0]) + __uint_as_float(t[1]);
}
DI float swap16_add(float a, float b) {
  auto t = __builtin_amdgcn_permlane16_swap(__float_as_uint(a), __float_as_uint(b), false, false);
  return __uint_as_float(t[0]) + __uint_as_float(t[1]);
}
DI v4u ldnt(const u16* p) { return __builtin_nontemporal_load(reinterpret_cast<const v4u*>(p)); }
DI void wg_barrier() { asm volatile("s_waitcnt lgkmcnt(0)\n\ts_barrier" ::: "memory"); }
DI void glds16_nt(const void* gsrc, unsigned lds_dst) {
  unsigned keep;
  asm volatile("s_mov_b32 %0, m0\n\ts_mov_b32 m0, %2\n\ts_nop 0\n\tglobal_load_lds_dwordx4 %1, off nt\n\ts_mov_b32 m0, %0"
               : "=&s"(keep) : "v"(gsrc), "s"(lds_dst) : "memory");
}
DI void glds16_def(const void* gsrc, unsigned lds_dst) {
  unsigned keep;
  asm volatile("s_mov_b32 %0, m0\n\ts_mov_b32 m0, %2\n\ts_nop 0\n\tglobal_load_lds_dwordx4 %1, off" SHPOL "\n\ts_mov_b32 m0, %0"
               : "=&s"(keep) : "v"(gsrc), "s"(lds_dst) : "memory");
}
typedef __amdgpu_buffer_rsrc_t rsrc_t;
DI rsrc_t mk_rsrc(const void* base, unsigned bytes) {
  const u64 b = (u64)base;
  const unsigned lo = __builtin_amdgcn_readfirstlane((unsigned)b), hi = __builtin_amdgcn_readfirstlane((unsigned)(b >> 32));
  return __builtin_amdgcn_make_buffer_rsrc((void*)(((u64)hi << 32) | lo), 0, bytes, 0x00020000);
}
DI v4u ldnt_b(rsrc_t r, unsigned voff, unsigned soff) { return __builtin_amdgcn_raw_buffer_load_b128(r, voff, soff, 2); }
DI v4u ld16(rsrc_t r, unsigned off) { return __builtin_amdgcn_raw_buffer_load_b128(r, off, 0, 16); }
DI void st8(rsrc_t r, unsigned off, unsigned lo, unsigned hi) { v2u v = {lo, hi}; __builtin_amdgcn_raw_buffer_store_b64(v, r, off, 0, 16); }
DI void st16(rsrc_t r, unsigned off, v4u v) { __builtin_amdgcn_raw_buffer_store_b128(v, r, off, 0, 16); }

struct Tag { unsigned hi, lo8, t32, top; };
DI unsigned g_lo(float v, const Tag& t) { return ((__float_as_uint(v) + 0x80u) >> 8) | (t.lo8 << 24); }
DI float g_val(unsigned lo) { return __uint_as_float(lo << 8); }
DI bool g_ok(unsigned lo, unsigned hi, const Tag& t) { return hi == t.hi && (lo >> 24) == t.lo8; }
DI bool g_ok2(const v4u& v, const Tag& t) { return g_ok(v.x, v.y, t) && g_ok(v.z, v.w, t); }
DI float round24(float v) { return __uint_as_float((__float_as_uint(v) + 0x80u) & 0xffffff00u); }
DI unsigned swz(unsigned r) { return ((r & 3u) << 2) | ((r >> 2) & 3u); }

#define STAMP(k) do { } while (0)

template <typename T> DI void lds_st(unsigned addr, T v) { *(LAS T*)(uintptr_t)addr = v; }
template <typename T> DI T lds_ld(unsigned addr) { return *(LAS T*)(uintptr_t)addr; }

typedef float f32x4v __attribute__((ext_vector_type(4)));

DI void dma_slice(const u16* src0, size_t row_stride, unsigned dst, int w, int lane) {
  asm volatile("" : "+v"(lane));
  const int g = lane >> 4, c16 = lane & 15;
  const u16* s = src0 + (size_t)(16 * w + g) * row_stride;
#pragma unroll
  for (int i = 0; i < 4; ++i) glds16_nt(s + (size_t)(4 * i) * row_stride + 8 * (c16 ^ ((g << 2) | i)), dst + (16 * w + 4 * i) * 256);
}
DI f32x4v gemv_mfma(const bf16x8 (&a)[4], unsigned wbase, int w, int lane) {
  const unsigned g = lane >> 4, q = (lane & 15) >> 2, pp = lane & 3;
  const unsigned chn = 2 * w + (pp >> 1);
  const unsigned a0 = wbase + (8 * g + q) * 256 + 16 * (chn ^ ((q << 2) | ((2 * g) & 3))) + 8 * (pp & 1);
  const unsigned a1 = wbase + (8 * g + 4 + q) * 256 + 16 * (chn ^ ((q << 2) | ((2 * g + 1) & 3))) + 8 * (pp & 1);
  s16x4 x[8];
  asm volatile("ds_read_b64_tr_b16 %0, %8\n\tds_read_b64_tr_b16 %1, %9\n\t"
               "ds_read_b64_tr_b16 %2, %8 offset:8192\n\tds_read_b64_tr_b16 %3, %9 offset:8192\n\t"
               "ds_read_b64_tr_b16 %4, %8 offset:16384\n\tds_read_b64_tr_b16 %5, %9 offset:16384\n\t"
               "ds_read_b64_tr_b16 %6, %8 offset:24576\n\tds_read_b64_tr_b16 %7, %9 offset:24576\n\ts_waitcnt lgkmcnt(0)"
               : "=&v"(x[0]), "=&v"(x[1]), "=&v"(x[2]), "=&v"(x[3]), "=&v"(x[4]), "=&v"(x[5]), "=&v"(x[6]), "=&v"(x[7])
               : "v"(a0), "v"(a1) : "memory");
  f32x4v d = {0.f, 0.f, 0.f, 0.f};
#pragma unroll
  for (int s = 0; s < 4; ++s)
    d = __builtin_amdgcn_mfma_f32_16x16x32_bf16(a[s], __builtin_shufflevector(x[2 * s], x[2 * s + 1], 0, 1, 2, 3, 4, 5, 6, 7), d, 0, 0, 0);
  return d;
}

template <int I0, int I1, bool NT = true> DI void dma_rows1k(const u16* src, unsigned dst, int w, int lane) {
  asm volatile("" : "+v"(lane));
  const u16* s = src + (size_t)(16 * w) * 512;
#pragma unroll
  for (int i = I0; i < I1; ++i) {
    if (NT) glds16_nt(s + i * 512 + 8 * (lane ^ swz(i)), dst + (16 * w + i) * 1024);
    else glds16_def(s + i * 512 + 8 * (lane ^ swz(i)), dst + (16 * w + i) * 1024);
  }
}
DI void vec_frags(bf16x8 (&a)[4], unsigned vbase, int lane) {
  const unsigned ad = vbase + (lane & 1) * 256 + (lane >> 4) * 16;
#pragma unroll
  for (int s = 0; s < 4; ++s) a[s] = lds_ld<bf16x8>(ad + 64 * s);
}
DI void gemv_publish(rsrc_t rs, const Tag& tg, const f32x4v& d, unsigned base, unsigned bstride, int w, int lane) {
  if (lane < 32) {
    const float v = lane < 16 ? d[0] : d[1];
    st8(rs, base + ((lane >> 4) * bstride + 16 * w + (lane & 15)) * 8, g_lo(v, tg), tg.hi);
  }
}
DI unsigned gather4(rsrc_t rs, const Tag& tg, unsigned off, unsigned pstride) {
  v4u v[4];
#pragma unroll
  for (int pj = 0; pj < 4; ++pj) v[pj] = ld16(rs, off + pj * pstride);
  unsigned spins = 0;
  for (;;) {
    bool bad[4], any = false;
#pragma unroll
    for (int pj = 0; pj < 4; ++pj) { bad[pj] = !__all(g_ok2(v[pj], tg)); any = any || bad[pj]; }
    if (!any || ++spins >= SPIN_CAP) break;
    asm volatile("" ::: "memory");
#pragma unroll
    for (int pj = 0; pj < 4; ++pj) if (bad[pj]) v[pj] = ld16(rs, off + pj * pstride);
  }
  return pk_bf((g_val(v[0].x) + g_val(v[1].x)) + (g_val(v[2].x) + g_val(v[3].x)), (g_val(v[0].z) + g_val(v[1].z)) + (g_val(v[2].z) + g_val(v[3].z)));
}
DI Tag make_tag(u64 E) {
  const u64 T = E + 1;
  Tag tg;
  tg.hi = __builtin_amdgcn_readfirstlane((unsigned)(T >> 8));
  tg.lo8 = __builtin_amdgcn_readfirstlane((unsigned)T & 0xffu);
  tg.t32 = __builtin_amdgcn_readfirstlane((unsigned)T);
  tg.top = __builtin_amdgcn_readfirstlane((unsigned)(T >> 32));
  return tg;
}

DI void stream_wg(const Params& p, rsrc_t rs, const u64 E, int h, int j, unsigned lb) {
  const int b = j >> 1, half = j & 1, bh = b * 32 + h;
  const int tid = threadIdx.x, lane = tid & 63, w = __builtin_amdgcn_readfirstlane(tid >> 6), c16 = lane & 15;
  const size_t off = ((size_t)bh * 8192 + 6144 + half * 1024 + w * 128) * 128;
  const rsrc_t rk = mk_rsrc(p.kc + off, 32768), rv = mk_rsrc(p.vc + off, 32768);
  dma_slice(p.wqB + (size_t)(128 * j) * 4096 + h * 128, 4096, lb + 8192, w, lane);
  bf16x8 xa[4];
#pragma unroll
  for (int s = 0; s < 4; ++s) xa[s] = *reinterpret_cast<const bf16x8*>(p.xB + (lane & 1) * 512 + 128 * j + 32 * s + 8 * (lane >> 4));
  v4u K[32], V[32];
#pragma unroll
  for (int i = 0; i < KINIT; ++i) K[i] = ldnt_b(rk, lane * 16, i * 1024);
  asm volatile("s_waitcnt vmcnt(%0)" :: "i"(KINIT) : "memory");
  wg_barrier();
  const Tag tg = make_tag(E);
  {
    const f32x4v d = gemv_mfma(xa, lb + 8192, w, lane);
    gemv_publish(rs, tg, d, WS_Q1 + (((unsigned)h * 4 + j) * 2 + 1) * 2048, 128, w, lane);
  }
  STAMP(1);
  v4u qa[4];
  const unsigned qoff = WS_Q1 + ((((unsigned)h * 4) * 2 + 1) * 2 + b) * 1024 + lane * 16;
#pragma unroll
  for (int i = KINIT; i < 32 + VPRE; ++i) {
    if (i < 32) K[i] = ldnt_b(rk, lane * 16, i * 1024); else V[i - 32] = ldnt_b(rv, lane * 16, (i - 32) * 1024);
    if (i == 24) { if (w == 0) {
#pragma unroll
        for (int pj = 0; pj < 4; ++pj) qa[pj] = ld16(rs, qoff + pj * 4096); } }
    if (i < KINIT + KSLOW) asm volatile("s_waitcnt vmcnt(%0)" :: "i"(SWIN) : "memory");
    else if (i >= KWIN) asm volatile("s_waitcnt vmcnt(%0)" :: "i"(KWIN) : "memory");
  }
  if (w == 0) {
    unsigned spins = 0;
    while (!__all(g_ok2(qa[0], tg) && g_ok2(qa[1], tg) && g_ok2(qa[2], tg) && g_ok2(qa[3], tg)) && ++spins < SPIN_CAP) {
      asm volatile("" ::: "memory");
#pragma unroll
      for (int pj = 0; pj < 4; ++pj) qa[pj] = ld16(rs, qoff + pj * 4096);
    }
    const float q0 = (g_val(qa[0].x) + g_val(qa[1].x)) + (g_val(qa[2].x) + g_val(qa[3].x));
    const float q1 = (g_val(qa[0].z) + g_val(qa[1].z)) + (g_val(qa[2].z) + g_val(qa[3].z));
    lds_st<unsigned>(lb + lane * 4, pk_bf(q0, q1));
  }
  wg_barrier();
  STAMP(2);
  const v4u q = lds_ld<v4u>(lb + c16 * 16);
  float s[32];
#pragma unroll
  for (int i = 0; i < 32; ++i) {
    float d = dot2(K[i].x, q.x, 0.f); d = dot2(K[i].y, q.y, d); d = dot2(K[i].z, q.z, d); d = dot2(K[i].w, q.w, d);
    if (i + VPRE < 32) V[i + VPRE] = ldnt_b(rv, lane * 16, (i + VPRE) * 1024);
    s[i] = d;
  }
  float m = -INFINITY;
#pragma unroll
  for (int i = 0; i < 32; ++i) { s[i] = rbf(rbf(row16_sum(s[i])) * SCALE); m = fmaxf(m, s[i]); }
  m = xrow_max(m);
  float l = 0.f, acc[8];
#pragma unroll
  for (int k = 0; k < 8; ++k) acc[k] = 0.f;
  const float mneg = -m * LOG2E;
#pragma unroll
  for (int i = 0; i < 32; ++i) { s[i] = __builtin_amdgcn_exp2f(fmaf(s[i], LOG2E, mneg)); l += s[i]; }
  l = xrow_sum(l);
  const unsigned ST = lb + 512, mg = lb + 1024;
  if (lane == 0) lds_st<v2u>(ST + w * 8, v2u{__float_as_uint(m), __float_as_uint(l)});
  wg_barrier();
  float M, L;
  {
    const f32x4 s0 = lds_ld<f32x4>(ST), s1 = lds_ld<f32x4>(ST + 16), s2 = lds_ld<f32x4>(ST + 32), s3 = lds_ld<f32x4>(ST + 48);
    M = fmaxf(fmaxf(fmaxf(s0[0], s0[2]), fmaxf(s1[0], s1[2])), fmaxf(fmaxf(s2[0], s2[2]), fmaxf(s3[0], s3[2])));
    L = __builtin_amdgcn_exp2f((s0[0] - M) * LOG2E) * s0[1] + __builtin_amdgcn_exp2f((s0[2] - M) * LOG2E) * s0[3]
      + __builtin_amdgcn_exp2f((s1[0] - M) * LOG2E) * s1[1] + __builtin_amdgcn_exp2f((s1[2] - M) * LOG2E) * s1[3]
      + __builtin_amdgcn_exp2f((s2[0] - M) * LOG2E) * s2[1] + __builtin_amdgcn_exp2f((s2[2] - M) * LOG2E) * s2[3]
      + __builtin_amdgcn_exp2f((s3[0] - M) * LOG2E) * s3[1] + __builtin_amdgcn_exp2f((s3[2] - M) * LOG2E) * s3[3];
  }
  const float ew = __builtin_amdgcn_exp2f((m - M) * LOG2E);
  if (tid == 0) st16(rs, WS_REC + (((unsigned)h * 4 + j) * 136 + 128) * 8, v4u{g_lo(L, tg), tg.hi, g_lo(M, tg), tg.hi});
  STAMP(3);
#pragma unroll
  for (int i = 0; i < 32; ++i) {
#pragma unroll
    for (int k = 0; k < 4; ++k) { acc[2 * k] = fmaf(s[i], bflo(V[i][k]), acc[2 * k]); acc[2 * k + 1] = fmaf(s[i], bfhi(V[i][k]), acc[2 * k + 1]); }
  }
  float r[4], t[2];
#pragma unroll
  for (int k = 0; k < 4; ++k) r[k] = swap32_add(acc[k], acc[k + 4]);
#pragma unroll
  for (int k = 0; k < 2; ++k) t[k] = swap16_add(r[k], r[k + 2]) * ew;
  {
    const int g = lane >> 4;
    lds_st<v2u>(mg + w * 512 + (8 * c16 + 4 * (g >> 1) + 2 * (g & 1)) * 4, v2u{__float_as_uint(t[0]), __float_as_uint(t[1])});
  }
  wg_barrier();
  STAMP(4);
  if (tid < 128) {
    float o = 0.f;
#pragma unroll
    for (int ww = 0; ww < 8; ++ww) o += lds_ld<float>(mg + ww * 512 + tid * 4);
    st8(rs, WS_REC + (((unsigned)h * 4 + j) * 136 + tid) * 8, g_lo(o, tg), tg.hi);
  }
  STAMP(5);
}

DI v4u poll_pair(rsrc_t rs, unsigned off, const Tag& tg) {
  v4u v = ld16(rs, off);
  unsigned spins = 0;
  while (!__all(g_ok2(v, tg)) && ++spins < SPIN_CAP) { __builtin_amdgcn_s_sleep(1); asm volatile("" ::: "memory"); v = ld16(rs, off); }
  return v;
}

DI void final_reduce(const Params& p, rsrc_t rs, const Tag& tg, int which, int a, int lane) {
  const int b = a >> 6, n0 = (a & 63) * 8, head = lane >> 1, hf = lane & 1;
  const unsigned off = WS_Y + ((((unsigned)which * 32 + head) * 2 + b) * 512 + n0 + 4 * hf) * 8;
  v4u v0 = ld16(rs, off), v1 = ld16(rs, off + 16);
  unsigned spins = 0;
  while (!(g_ok2(v0, tg) && g_ok2(v1, tg)) && ++spins < SPIN_CAP) { __builtin_amdgcn_s_sleep(1); asm volatile("" ::: "memory"); v0 = ld16(rs, off); v1 = ld16(rs, off + 16); }
  float r0 = swap32_add(g_val(v0.x), g_val(v1.x)), r1 = swap32_add(g_val(v0.z), g_val(v1.z));
  float x = swap16_add(r0, r1);
  x += dpp<0x4E>(x);
  x += dpp<0x124>(x);
  x += dpp<0x128>(x);
  if ((lane & 15) < 2) p.out[b * 1024 + which * 512 + n0 + 4 * hf + (lane >> 4)] = (u16)f2bf_bits(x);
}

DI void chain_wg(const Params& p, rsrc_t rs, const u64 E, int a, unsigned lb) {
  const int h = a >> 2, j = a & 3;
  const int tid = threadIdx.x, lane = tid & 63, w = __builtin_amdgcn_readfirstlane(tid >> 6), g = lane >> 4, c16 = lane & 15;
  const unsigned scr = lb + SCR;
  dma_slice(p.wqA + (size_t)(128 * j) * 4096 + h * 128, 4096, scr, w, lane);
  bf16x8 fa[4], WK[4];
#pragma unroll
  for (int s = 0; s < 4; ++s) fa[s] = *reinterpret_cast<const bf16x8*>(p.xA + (lane & 1) * 512 + 128 * j + 32 * s + 8 * g);
  {
    const rsrc_t rw = mk_rsrc(p.wuk + (size_t)(128 * j + 16 * w) * 4096 + h * 128, 16 * 8192);
#pragma unroll
    for (int s = 0; s < 4; ++s) WK[s] = __builtin_bit_cast(bf16x8, ldnt_b(rw, c16 * 8192 + g * 16, 64 * s));
  }
  const int bA = a >> 6;
  asm volatile("s_barrier" ::: "memory");
  dma_rows1k<0, CK0>(p.ckv + (size_t)a * 128 * 512, lb, w, lane);
  asm volatile("s_waitcnt vmcnt(%0)" :: "i"(4 + CK0) : "memory");
  wg_barrier();
  const Tag tg = make_tag(E);
  {
    const f32x4v d = gemv_mfma(fa, scr, w, lane);
    gemv_publish(rs, tg, d, WS_Q1 + (((unsigned)h * 4 + j) * 2 + 0) * 2048, 128, w, lane);
  }
  STAMP(1);
  dma_rows1k<CK0, CK1>(p.ckv + (size_t)a * 128 * 512, lb, w, lane);
  wg_barrier();
  if (tid < 128) {
    const unsigned v = gather4(rs, tg, WS_Q1 + (((unsigned)h * 4) * 4 + (tid >> 6)) * 1024 + (tid & 63) * 16, 4096);
    lds_st<unsigned>(scr + tid * 4, v);
  }
  wg_barrier();
  STAMP(2);
  {
    bf16x8 qf[4];
    vec_frags(qf, scr, lane);
    f32x4v d = {0.f, 0.f, 0.f, 0.f};
#pragma unroll
    for (int s = 0; s < 4; ++s) d = __builtin_amdgcn_mfma_f32_16x16x32_bf16(WK[s], qf[s], d, 0, 0, 0);
    if (c16 < 2)
      st16(rs, WS_QL + (((unsigned)c16 * 32 + h) * 256 + 64 * j + 8 * w + 2 * g) * 8, v4u{pk_bf(d[0], d[1]), tg.t32, pk_bf(d[2], d[3]), tg.t32});
  }
  STAMP(3);
  dma_rows1k<CK1, 16>(p.ckv + (size_t)a * 128 * 512, lb, w, lane);
  const int r32 = lane & 31, hh = lane >> 5;
  float sc[8];
  s16x4 xt[32];
  bf16x8 ka[16];
  const unsigned fr = swz(r32);
  {
    v4u v[8];
    const unsigned base = WS_QL + (unsigned)bA * 65536 + tid * 16;
#pragma unroll
    for (int i = 0; i < 8; ++i) { v[i] = ld16(rs, base + i * 8192); }
    asm volatile("s_waitcnt vmcnt(8)" ::: "memory");
    wg_barrier();
    STAMP(11);
    {
      const unsigned abase = lb + (32 * (w & 3) + r32) * 1024 + 512 * (w >> 2);
#pragma unroll
      for (int s = 0; s < 16; ++s) ka[s] = lds_ld<bf16x8>(abase + 16 * ((unsigned)((2 * s + hh) & 15) ^ fr) + 256 * (s >> 3));
      const unsigned blk = (lane >> 4) & 1, q = (lane & 15) >> 2, pp = lane & 3;
      unsigned tb[4];
#pragma unroll
      for (int mt = 0; mt < 2; ++mt)
#pragma unroll
        for (int t = 0; t < 2; ++t) {
          const unsigned row = 8 * hh + 4 * t + q, chn = 8 * w + 4 * mt + 2 * blk + (pp >> 1);
          tb[mt * 2 + t] = lb + row * 1024 + 16 * (chn ^ swz(row)) + 8 * (pp & 1);
        }
#pragma unroll
      for (int hf = 0; hf < 2; ++hf) {
        asm volatile(
            "ds_read_b64_tr_b16 %0, %16\n\tds_read_b64_tr_b16 %1, %17\n\tds_read_b64_tr_b16 %2, %18\n\tds_read_b64_tr_b16 %3, %19\n\t"
            "ds_read_b64_tr_b16 %4, %16 offset:16384\n\tds_read_b64_tr_b16 %5, %17 offset:16384\n\tds_read_b64_tr_b16 %6, %18 offset:16384\n\tds_read_b64_tr_b16 %7, %19 offset:16384\n\t"
            "ds_read_b64_tr_b16 %8, %16 offset:32768\n\tds_read_b64_tr_b16 %9, %17 offset:32768\n\tds_read_b64_tr_b16 %10, %18 offset:32768\n\tds_read_b64_tr_b16 %11, %19 offset:32768\n\t"
            "ds_read_b64_tr_b16 %12, %16 offset:49152\n\tds_read_b64_tr_b16 %13, %17 offset:49152\n\tds_read_b64_tr_b16 %14, %18 offset:49152\n\tds_read_b64_tr_b16 %15, %19 offset:49152\n\t"
            "s_waitcnt lgkmcnt(0)"
            : "=&v"(xt[16 * hf + 0]), "=&v"(xt[16 * hf + 1]), "=&v"(xt[16 * hf + 2]), "=&v"(xt[16 * hf + 3]),
              "=&v"(xt[16 * hf + 4]), "=&v"(xt[16 * hf + 5]), "=&v"(xt[16 * hf + 6]), "=&v"(xt[16 * hf + 7]),
              "=&v"(xt[16 * hf + 8]), "=&v"(xt[16 * hf + 9]), "=&v"(xt[16 * hf + 10]), "=&v"(xt[16 * hf + 11]),
              "=&v"(xt[16 * hf + 12]), "=&v"(xt[16 * hf + 13]), "=&v"(xt[16 * hf + 14]), "=&v"(xt[16 * hf + 15])
            : "v"(tb[0] + hf * 65536), "v"(tb[1] + hf * 65536), "v"(tb[2] + hf * 65536), "v"(tb[3] + hf * 65536) : "memory");
      }
    }
    STAMP(12);
    unsigned spins = 0;
    const unsigned cq = tid & 127, h0 = tid >> 7;
    for (;;) {
      bool bad[8], any = false;
#pragma unroll
      for (int i = 0; i < 8; ++i) { bad[i] = !__all(v[i].y == tg.t32 && v[i].w == tg.t32); any = any || bad[i]; }
      if (!any || ++spins >= SPIN_CAP) break;
      asm volatile("" ::: "memory");
#pragma unroll
      for (int i = 0; i < 8; ++i) if (bad[i]) v[i] = ld16(rs, base + i * 8192);
    }
#pragma unroll
    for (int i = 0; i < 8; ++i) {
      const unsigned head = 4 * i + h0;
      lds_st<v2u>(scr + head * 1024 + 16 * ((cq >> 1) ^ swz(head)) + 8 * (cq & 1), v2u{v[i].x, v[i].z});
    }
  }
  wg_barrier();
  STAMP(4);
  dma_slice(p.wuv + (size_t)(128 * j) * 4096 + h * 128, 4096, lb, w, lane);
  {
    const int ch = w >> 2;
    f32x16 acc;
#pragma unroll
    for (int i = 0; i < 16; ++i) acc[i] = 0.f;
    const unsigned bbase = scr + r32 * 1024 + 512 * ch;
#pragma unroll
    for (int s = 0; s < 16; ++s) {
      const bf16x8 bv = lds_ld<bf16x8>(bbase + 16 * ((unsigned)((2 * s + hh) & 15) ^ fr) + 256 * (s >> 3));
      acc = __builtin_amdgcn_mfma_f32_32x32x16_bf16(ka[s], bv, acc, 0, 0, 0);
      if ((s & 7) == 7) __builtin_amdgcn_sched_barrier(0);
    }
    f32x4 s0, s1;
#pragma unroll
    for (int k = 0; k < 4; ++k) { s0[k] = ch ? acc[k] : acc[8 + k]; s1[k] = ch ? acc[4 + k] : acc[12 + k]; }
    const unsigned XB = lb + 32768;
    lds_st<f32x4>(XB + w * 2048 + lane * 16, s0);
    lds_st<f32x4>(XB + w * 2048 + 1024 + lane * 16, s1);
    wg_barrier();
    const f32x4 t0 = lds_ld<f32x4>(XB + (w ^ 4) * 2048 + lane * 16), t1 = lds_ld<f32x4>(XB + (w ^ 4) * 2048 + 1024 + lane * 16);
#pragma unroll
    for (int k = 0; k < 4; ++k) { sc[k] = ((ch ? acc[8 + k] : acc[k]) + t0[k]) * SCALE; sc[4 + k] = ((ch ? acc[12 + k] : acc[4 + k]) + t1[k]) * SCALE; }
  }
  const unsigned MX = scr + 16384, LS = scr + 17408, PB = scr + 18432;
  float Mh;
  {
    float m = sc[0];
#pragma unroll
    for (int k = 1; k < 8; ++k) m = fmaxf(m, sc[k]);
    { auto t = __builtin_amdgcn_permlane32_swap(__float_as_uint(m), __float_as_uint(m), false, false); m = fmaxf(__uint_as_float(t[0]), __uint_as_float(t[1])); }
    if (lane < 32) lds_st<float>(MX + (lane * 8 + w) * 4, m);
    wg_barrier();
    const f32x4 ma = lds_ld<f32x4>(MX + r32 * 32), mb = lds_ld<f32x4>(MX + r32 * 32 + 16);
    float M = fmaxf(fmaxf(fmaxf(ma[0], ma[1]), fmaxf(ma[2], ma[3])), fmaxf(fmaxf(mb[0], mb[1]), fmaxf(mb[2], mb[3])));
    M = round24(M);
    Mh = M;
    const float mneg = -M * LOG2E;
    unsigned pk[4]; float l = 0.f;
#pragma unroll
    for (int k = 0; k < 4; ++k) {
      const float p0 = __builtin_amdgcn_exp2f(fmaf(sc[2 * k], LOG2E, mneg)), p1 = __builtin_amdgcn_exp2f(fmaf(sc[2 * k + 1], LOG2E, mneg));
      pk[k] = pk_bf(p0, p1);
      l += bflo(pk[k]) + bfhi(pk[k]);
    }
    { auto t = __builtin_amdgcn_permlane32_swap(__float_as_uint(l), __float_as_uint(l), false, false); l = __uint_as_float(t[0]) + __uint_as_float(t[1]); }
    if (lane < 32) lds_st<float>(LS + (lane * 8 + w) * 4, l);
    const int kt = w & 3, ch = w >> 2;
    lds_st<v2u>(PB + r32 * 272 + (32 * kt + 16 * ch + 4 * hh) * 2, v2u{pk[0], pk[1]});
    lds_st<v2u>(PB + r32 * 272 + (32 * kt + 16 * ch + 8 + 4 * hh) * 2, v2u{pk[2], pk[3]});
  }
  wg_barrier();
  f32x16 o0, o1;
#pragma unroll
  for (int i = 0; i < 16; ++i) { o0[i] = 0.f; o1[i] = 0.f; }
#pragma unroll
  for (int s = 0; s < 8; ++s) {
    const bf16x8 pv = lds_ld<bf16x8>(PB + r32 * 272 + 32 * s + 16 * hh);
    o0 = __builtin_amdgcn_mfma_f32_32x32x16_bf16(__builtin_shufflevector(xt[4 * s], xt[4 * s + 1], 0, 1, 2, 3, 4, 5, 6, 7), pv, o0, 0, 0, 0);
    o1 = __builtin_amdgcn_mfma_f32_32x32x16_bf16(__builtin_shufflevector(xt[4 * s + 2], xt[4 * s + 3], 0, 1, 2, 3, 4, 5, 6, 7), pv, o1, 0, 0, 0);
  }
  STAMP(5);
  {
    const unsigned tb = lb + 32768 + w * 4096;
#pragma unroll
    for (int gq = 0; gq < 4; ++gq) {
      lds_st<v2u>(tb + r32 * 128 + 16 * (gq ^ (r32 & 7)) + 8 * hh, v2u{pk_bf(o0[4 * gq], o0[4 * gq + 1]), pk_bf(o0[4 * gq + 2], o0[4 * gq + 3])});
      lds_st<v2u>(tb + r32 * 128 + 16 * ((4 + gq) ^ (r32 & 7)) + 8 * hh, v2u{pk_bf(o1[4 * gq], o1[4 * gq + 1]), pk_bf(o1[4 * gq + 2], o1[4 * gq + 3])});
    }
    asm volatile("s_waitcnt lgkmcnt(0)" ::: "memory");
#pragma unroll
    for (int i = 0; i < 4; ++i) {
      const int head = 8 * i + (lane >> 3);
      const v4u v = lds_ld<v4u>(tb + head * 128 + 16 * ((lane & 7) ^ (head & 7)));
      st16(rs, WS_PART + (((unsigned)a * 32 + head) * 512 + 64 * w + 8 * (lane & 7)) * 2, v);
    }
    dma_slice(p.woA + (size_t)(h * 128) * 512 + 128 * j, 512, lb + 65536, w, lane);
    asm volatile("s_waitcnt vmcnt(4)" ::: "memory");
  }
  wg_barrier();
  if (w == 0 && lane < 32) {
    const f32x4 la = lds_ld<f32x4>(LS + lane * 32), lc = lds_ld<f32x4>(LS + lane * 32 + 16);
    const float l = ((la[0] + la[1]) + (la[2] + la[3])) + ((lc[0] + lc[1]) + (lc[2] + lc[3]));
    st16(rs, WS_ML + ((unsigned)lane * 128 + a) * 16, v4u{g_lo(Mh, tg), tg.hi, g_lo(l, tg), tg.hi});
  }
  STAMP(6);
  const unsigned MLS = scr, R2 = scr + 1024, V16 = scr + 5120;
  if (tid < 128) {
    const v4u v = poll_pair(rs, WS_ML + ((unsigned)h * 128 + tid) * 16, tg);
    lds_st<v2u>(MLS + tid * 8, v2u{v.x << 8, v.z << 8});
  }
  wg_barrier();
  STAMP(7);
  {
    v4u pv[4];
#pragma unroll
    for (int i = 0; i < 4; ++i) pv[i] = ld16(rs, WS_PART + (((unsigned)(16 * w + 4 * i + g) * 32 + h) * 512 + 128 * j + 8 * c16) * 2);
    const int bb = w >> 2;
    const v2u ml = lds_ld<v2u>(MLS + (bb * 64 + lane) * 8);
    const float m = __uint_as_float(ml.x), l = __uint_as_float(ml.y);
    float M = m;
    M = fmaxf(M, dpp<0xB1>(M)); M = fmaxf(M, dpp<0x4E>(M)); M = fmaxf(M, dpp<0x141>(M)); M = fmaxf(M, dpp<0x140>(M)); M = xrow_max(M);
    const float e = __builtin_amdgcn_exp2f((m - M) * LOG2E);
    const float L = xrow_sum(row16_sum(e * l));
    const float wt = e / L;
    float wi[4];
#pragma unroll
    for (int i = 0; i < 4; ++i) wi[i] = __shfl(wt, (16 * w + 4 * i + g) & 63);
    float acc[8];
#pragma unroll
    for (int k = 0; k < 8; ++k) acc[k] = 0.f;
#pragma unroll
    for (int i = 0; i < 4; ++i) {
#pragma unroll
      for (int k = 0; k < 4; ++k) { acc[2 * k] = fmaf(wi[i], bflo(pv[i][k]), acc[2 * k]); acc[2 * k + 1] = fmaf(wi[i], bfhi(pv[i][k]), acc[2 * k + 1]); }
    }
    float r[4], t[2];
#pragma unroll
    for (int k = 0; k < 4; ++k) r[k] = swap32_add(acc[k], acc[k + 4]);
#pragma unroll
    for (int k = 0; k < 2; ++k) t[k] = swap16_add(r[k], r[k + 2]);
    lds_st<v2u>(R2 + (w * 128 + 8 * c16 + 4 * (g >> 1) + 2 * (g & 1)) * 4, v2u{__float_as_uint(t[0]), __float_as_uint(t[1])});
  }
  dma_slice(p.woB + (size_t)(h * 128) * 512 + 128 * j, 512, lb + 98304, w, lane);
  wg_barrier();
  if (tid < 128) {
    const int bb = tid >> 6, cp = tid & 63;
    float x0 = 0.f, x1 = 0.f;
#pragma unroll
    for (int ww = 0; ww < 4; ++ww) { const v2u t = lds_ld<v2u>(R2 + ((4 * bb + ww) * 128 + 2 * cp) * 4); x0 += __uint_as_float(t.x); x1 += __uint_as_float(t.y); }
    lds_st<unsigned>(V16 + tid * 4, pk_bf(x0, x1));
  }
  wg_barrier();
  {
    bf16x8 f[4];
    vec_frags(f, V16, lane);
    const f32x4v d = gemv_mfma(f, lb, w, lane);
    gemv_publish(rs, tg, d, WS_C4 + ((unsigned)h * 4 + j) * 2048, 128, w, lane);
  }
  STAMP(8);
  if (tid < 128) {
    const unsigned v = gather4(rs, tg, WS_C4 + ((unsigned)h * 4) * 2048 + (tid >> 6) * 1024 + (tid & 63) * 16, 2048);
    lds_st<unsigned>(V16 + 512 + tid * 4, v);
  }
  wg_barrier();
  {
    bf16x8 f[4];
    vec_frags(f, V16 + 512, lane);
    const f32x4v d = gemv_mfma(f, lb + 65536, w, lane);
    gemv_publish(rs, tg, d, WS_Y + (((unsigned)0 * 32 + h) * 1024 + 128 * j) * 8, 512, w, lane);
  }
  STAMP(9);
  asm volatile("s_waitcnt vmcnt(0)" ::: "memory");
  if (w == 7) final_reduce(p, rs, tg, 0, a, lane);
  if (tid < 128) {
    const int bb = tid >> 6, dp = tid & 63;
    const unsigned ra = WS_REC + (((unsigned)h * 4 + 2 * bb) * 136) * 8, rb = ra + 136 * 8;
    v4u oa = ld16(rs, ra + dp * 16), ob = ld16(rs, rb + dp * 16), sa = ld16(rs, ra + 1024), sb = ld16(rs, rb + 1024);
    unsigned spins = 0;
    for (;;) {
      const bool ba = !__all(g_ok2(oa, tg)), bo = !__all(g_ok2(ob, tg)), bs = !__all(g_ok2(sa, tg) && g_ok2(sb, tg));
      if (!(ba || bo || bs) || ++spins >= SPIN_CAP) break;
      __builtin_amdgcn_s_sleep(2); asm volatile("" ::: "memory");
      if (ba) oa = ld16(rs, ra + dp * 16);
      if (bo) ob = ld16(rs, rb + dp * 16);
      if (bs) { sa = ld16(rs, ra + 1024); sb = ld16(rs, rb + 1024); }
    }
    const float ma = g_val(sa.z), mb = g_val(sb.z), M = fmaxf(ma, mb);
    const float ea = __builtin_amdgcn_exp2f((ma - M) * LOG2E), eb = __builtin_amdgcn_exp2f((mb - M) * LOG2E);
    const float inv = 1.f / (ea * g_val(sa.x) + eb * g_val(sb.x));
    lds_st<unsigned>(V16 + 1024 + tid * 4, pk_bf((ea * g_val(oa.x) + eb * g_val(ob.x)) * inv, (ea * g_val(oa.z) + eb * g_val(ob.z)) * inv));
  }
  wg_barrier();
  STAMP(10);
  {
    bf16x8 f[4];
    vec_frags(f, V16 + 1024, lane);
    const f32x4v d = gemv_mfma(f, lb + 98304, w, lane);
    gemv_publish(rs, tg, d, WS_Y + (((unsigned)1 * 32 + h) * 1024 + 128 * j) * 8, 512, w, lane);
  }
  STAMP(13);
  if (w == 0) {
    final_reduce(p, rs, tg, 1, a, lane);
    if (a == 0 && lane == 0) __hip_atomic_store(reinterpret_cast<u64*>(p.ws + WS_EPOCH), ((u64)tg.top << 32) | tg.t32, __ATOMIC_RELAXED, __HIP_MEMORY_SCOPE_AGENT);
  }
  STAMP(14);
}

__global__ void __launch_bounds__(512, 2) fused_decode_kernel(Params p) {
  extern __shared__ __attribute__((aligned(16))) char smem[];
  const unsigned lb = (unsigned)(uintptr_t)(LAS char*)smem;
  STAMP(0);
  u64 pc; asm volatile("s_getpc_b64 %0" : "=s"(pc));
  const rsrc_t rs = __builtin_amdgcn_make_buffer_rsrc(p.ws, 0, WS_END, 0x00020000);
  const u64 E = __hip_atomic_load(reinterpret_cast<u64*>(p.ws + WS_EPOCH), __ATOMIC_RELAXED, __HIP_MEMORY_SCOPE_AGENT);
  const int gidx = blockIdx.x;
  const int role = (gidx >> 3) & 1, k = gidx >> 4, head = 4 * (gidx & 7) + (k >> 2), j = k & 3;
  if (role == 0) {
    const unsigned o = (threadIdx.x * 64u < CODE_BYTES) ? threadIdx.x * 64u : 0u;
    const u64 ad = (pc & ~63ull) + o;
    unsigned keep;
    asm volatile("s_mov_b32 %0, m0\n\ts_mov_b32 m0, %2\n\ts_nop 0\n\tglobal_load_lds_dword %1, off\n\ts_mov_b32 m0, %0"
                 : "=&s"(keep) : "v"(ad), "s"(lb + 65536u + (unsigned)__builtin_amdgcn_readfirstlane(threadIdx.x >> 6) * 256u) : "memory");
    stream_wg(p, rs, E, head, j, lb);
  }
  else chain_wg(p, rs, E, head * 4 + j, lb);
}

extern "C" void kernel_launch(void* const* d_in, const int* in_sizes, int n_in, void* d_out, int out_size, void* d_ws, size_t ws_size,
                              hipStream_t stream) {
  Params p;
  p.xA = (const u16*)d_in[0]; p.ckv = (const u16*)d_in[1]; p.wqA = (const u16*)d_in[2]; p.wuk = (const u16*)d_in[3];
  p.wuv = (const u16*)d_in[4]; p.woA = (const u16*)d_in[5]; p.xB = (const u16*)d_in[6]; p.kc = (const u16*)d_in[7];
  p.vc = (const u16*)d_in[8]; p.wqB = (const u16*)d_in[9]; p.woB = (const u16*)d_in[10];
  p.out = (u16*)d_out; p.ws = (unsigned char*)d_ws;
  (void)hipFuncSetAttribute((const void*)fused_decode_kernel, hipFuncAttributeMaxDynamicSharedMemorySize, LDS_BYTES);
  fused_decode_kernel<<<256, 512, LDS_BYTES, stream>>>(p);
}
```

```cpp
#include <hip/hip_runtime.h>
#include <stdint.h>
#define SHPOL " nt"

typedef unsigned short u16;
typedef unsigned long long u64;
typedef unsigned v4u __attribute__((ext_vector_type(4)));
typedef unsigned v2u __attribute__((ext_vector_type(2)));
typedef float f32x4 __attribute__((ext_vector_type(4)));
typedef float f32x16 __attribute__((ext_vector_type(16)));
typedef short bf16x8 __attribute__((ext_vector_type(8)));
typedef short s16x4 __attribute__((ext_vector_type(4)));
typedef __bf16 bf16x2 __attribute__((ext_vector_type(2)));
#define LAS __attribute__((address_space(3)))
#define DI __device__ __forceinline__

constexpr unsigned WS_EPOCH = 0;
constexpr unsigned WS_Q1 = 4096;
constexpr unsigned WS_QL = WS_Q1 + 65536u * 8;
constexpr unsigned WS_ML = WS_QL + 16384u * 8;
constexpr unsigned WS_C4 = WS_ML + 8192u * 8;
constexpr unsigned WS_REC = WS_C4 + 32768u * 8;
constexpr unsigned WS_Y = WS_REC + 32u * 8 * 136 * 8;
constexpr unsigned WS_PART = WS_Y + 65536u * 8;
constexpr unsigned WS_STAMP = WS_PART + 128u * 32 * 512 * 2;
constexpr unsigned WS_END = WS_STAMP + 256u * 16 * 8;

constexpr unsigned CODE_BYTES = 16384;
constexpr int LDS_BYTES = 163840;
constexpr unsigned SCR = 131072;
constexpr unsigned SPIN_CAP = 1u << 16;
#define KINIT_ 1
#define KWIN_ 8
#define CK1_ 12
#define VPRE_ 8
#define KSLOW_ 3
#define SWIN_ 0
#define CK0_ 6
constexpr int CK0 = CK0_;
#define NY_ 20
constexpr int NY = NY_, NX = 32 - NY_;
constexpr int KSLOW = KSLOW_, SWIN = SWIN_;
constexpr int KINIT = KINIT_, KWIN = KWIN_, CK1 = CK1_, VPRE = VPRE_;
constexpr float SCALE = 0.08838834764831845f;
constexpr float LOG2E = 1.4426950408889634f;

struct Params {
  const u16 *xA, *ckv, *wqA, *wuk, *wuv, *woA, *xB, *kc, *vc, *wqB, *woB;
  u16* out;
  unsigned char* ws;
};

DI float bflo(unsigned w) { return __uint_as_float(w << 16); }
DI float bfhi(unsigned w) { return __uint_as_float(w & 0xffff0000u); }
DI unsigned f2bf_bits(float f) { return (unsigned)__builtin_bit_cast(unsigned short, (__bf16)f); }
DI float rbf(float f) { return __uint_as_float(f2bf_bits(f) << 16); }
DI unsigned pk_bf(float lo, float hi) { return f2bf_bits(lo) | (f2bf_bits(hi) << 16); }
DI float dot2(unsigned a, unsigned b, float acc) {
  return __builtin_amdgcn_fdot2_f32_bf16(__builtin_bit_cast(bf16x2, a), __builtin_bit_cast(bf16x2, b), acc, false);
}
template <int CTRL> DI float dpp(float x) {
  return __builtin_bit_cast(float, __builtin_amdgcn_mov_dpp(__builtin_bit_cast(int, x), CTRL, 0xf, 0xf, true));
}
DI float row16_sum(float x) { x += dpp<0xB1>(x); x += dpp<0x4E>(x); x += dpp<0x141>(x); x += dpp<0x140>(x); return x; }
DI float xrow_max(float x) {
  auto s = __builtin_amdgcn_permlane16_swap(__float_as_uint(x), __float_as_uint(x), false, false);
  x = fmaxf(__uint_as_float(s[0]), __uint_as_float(s[1]));
  auto t = __builtin_amdgcn_permlane32_swap(__float_as_uint(x), __float_as_uint(x), false, false);
  return fmaxf(__uint_as_float(t[0]), __uint_as_float(t[1]));
}
DI float xrow_sum(float x) {
  auto s = __builtin_amdgcn_permlane16_swap(__float_as_uint(x), __float_as_uint(x), false, false);
  x = __uint_as_float(s[0]) + __uint_as_float(s[1]);
  auto t = __builtin_amdgcn_permlane32_swap(__float_as_uint(x), __float_as_uint(x), false, false);
  return __uint_as_float(t[0]) + __uint_as_float(t[1]);
}
DI float swap32_add(float a, float b) {
  auto t = __builtin_amdgcn_permlane32_swap(__float_as_uint(a), __float_as_uint(b), false, false);
  return __uint_as_float(t[0]) + __uint_as_float(t[1]);
}
DI float swap16_add(float a, float b) {
  auto t = __builtin_amdgcn_permlane16_swap(__float_as_uint(a), __float_as_uint(b), false, false);
  return __uint_as_float(t[0]) + __uint_as_float(t[1]);
}
DI v4u ldnt(const u16* p) { return __builtin_nontemporal_load(reinterpret_cast<const v4u*>(p)); }
DI void wg_barrier() { asm volatile("s_waitcnt lgkmcnt(0)\n\ts_barrier" ::: "memory"); }
DI void glds16_nt(const void* gsrc, unsigned lds_dst) {
  unsigned keep;
  asm volatile("s_mov_b32 %0, m0\n\ts_mov_b32 m0, %2\n\ts_nop 0\n\tglobal_load_lds_dwordx4 %1, off nt\n\ts_mov_b32 m0, %0"
               : "=&s"(keep) : "v"(gsrc), "s"(lds_dst) : "memory");
}
DI void glds16_def(const void* gsrc, unsigned lds_dst) {
  unsigned keep;
  asm volatile("s_mov_b32 %0, m0\n\ts_mov_b32 m0, %2\n\ts_nop 0\n\tglobal_load_lds_dwordx4 %1, off" SHPOL "\n\ts_mov_b32 m0, %0"
               : "=&s"(keep) : "v"(gsrc), "s"(lds_dst) : "memory");
}
typedef __amdgpu_buffer_rsrc_t rsrc_t;
DI rsrc_t mk_rsrc(const void* base, unsigned bytes) {
  const u64 b = (u64)base;
  const unsigned lo = __builtin_amdgcn_readfirstlane((unsigned)b), hi = __builtin_amdgcn_readfirstlane((unsigned)(b >> 32));
  return __builtin_amdgcn_make_buffer_rsrc((void*)(((u64)hi << 32) | lo), 0, bytes, 0x00020000);
}
DI v4u ldnt_b(rsrc_t r, unsigned voff, unsigned soff) { return __builtin_amdgcn_raw_buffer_load_b128(r, voff, soff, 2); }
DI v4u ld16(rsrc_t r, unsigned off) { return __builtin_amdgcn_raw_buffer_load_b128(r, off, 0, 16); }
DI void st8(rsrc_t r, unsigned off, unsigned lo, unsigned hi) { v2u v = {lo, hi}; __builtin_amdgcn_raw_buffer_store_b64(v, r, off, 0, 16); }
DI void st16(rsrc_t r, unsigned off, v4u v) { __builtin_amdgcn_raw_buffer_store_b128(v, r, off, 0, 16); }

struct Tag { unsigned hi, lo8, t32, top, nz; };
DI unsigned g_lo(float v, const Tag& t) { return ((__float_as_uint(v) + 0x80u) >> 8) | (t.lo8 << 24); }
DI float g_val(unsigned lo) { return __uint_as_float(lo << 8); }
DI bool g_ok(unsigned lo, unsigned hi, const Tag& t) { return hi == t.hi && (lo >> 24) == t.lo8; }
DI bool g_ok2(const v4u& v, const Tag& t) { return g_ok(v.x, v.y, t) && g_ok(v.z, v.w, t); }
DI float round24(float v) { return __uint_as_float((__float_as_uint(v) + 0x80u) & 0xffffff00u); }
DI unsigned swz(unsigned r) { return ((r & 3u) << 2) | ((r >> 2) & 3u); }

#define STAMP(k) do { } while (0)

template <typename T> DI void lds_st(unsigned addr, T v) { *(LAS T*)(uintptr_t)addr = v; }
template <typename T> DI T lds_ld(unsigned addr) { return *(LAS T*)(uintptr_t)addr; }

typedef float f32x4v __attribute__((ext_vector_type(4)));

DI void dma_slice(const u16* src0, size_t row_stride, unsigned dst, int w, int lane) {
  asm volatile("" : "+v"(lane));
  const int g = lane >> 4, c16 = lane & 15;
  const u16* s = src0 + (size_t)(16 * w + g) * row_stride;
#pragma unroll
  for (int i = 0; i < 4; ++i) glds16_nt(s + (size_t)(4 * i) * row_stride + 8 * (c16 ^ ((g << 2) | i)), dst + (16 * w + 4 * i) * 256);
}
DI f32x4v gemv_mfma(const bf16x8 (&a)[4], unsigned wbase, int w, int lane) {
  const unsigned g = lane >> 4, q = (lane & 15) >> 2, pp = lane & 3;
  const unsigned chn = 2 * w + (pp >> 1);
  const unsigned a0 = wbase + (8 * g + q) * 256 + 16 * (chn ^ ((q << 2) | ((2 * g) & 3))) + 8 * (pp & 1);
  const unsigned a1 = wbase + (8 * g + 4 + q) * 256 + 16 * (chn ^ ((q << 2) | ((2 * g + 1) & 3))) + 8 * (pp & 1);
  s16x4 x[8];
  asm volatile("ds_read_b64_tr_b16 %0, %8\n\tds_read_b64_tr_b16 %1, %9\n\t"
               "ds_read_b64_tr_b16 %2, %8 offset:8192\n\tds_read_b64_tr_b16 %3, %9 offset:8192\n\t"
               "ds_read_b64_tr_b16 %4, %8 offset:16384\n\tds_read_b64_tr_b16 %5, %9 offset:16384\n\t"
               "ds_read_b64_tr_b16 %6, %8 offset:24576\n\tds_read_b64_tr_b16 %7, %9 offset:24576\n\ts_waitcnt lgkmcnt(0)"
               : "=&v"(x[0]), "=&v"(x[1]), "=&v"(x[2]), "=&v"(x[3]), "=&v"(x[4]), "=&v"(x[5]), "=&v"(x[6]), "=&v"(x[7])
               : "v"(a0), "v"(a1) : "memory");
  f32x4v d = {0.f, 0.f, 0.f, 0.f};
#pragma unroll
  for (int s = 0; s < 4; ++s)
    d = __builtin_amdgcn_mfma_f32_16x16x32_bf16(a[s], __builtin_shufflevector(x[2 * s], x[2 * s + 1], 0, 1, 2, 3, 4, 5, 6, 7), d, 0, 0, 0);
  return d;
}

template <int I0, int I1, bool NT = true> DI void dma_rows1k(const u16* src, unsigned dst, int w, int lane) {
  asm volatile("" : "+v"(lane));
  const u16* s = src + (size_t)(16 * w) * 512;
#pragma unroll
  for (int i = I0; i < I1; ++i) {
    if (NT) glds16_nt(s + i * 512 + 8 * (lane ^ swz(i)), dst + (16 * w + i) * 1024);
    else glds16_def(s + i * 512 + 8 * (lane ^ swz(i)), dst + (16 * w + i) * 1024);
  }
}
DI void vec_frags(bf16x8 (&a)[4], unsigned vbase, int lane) {
  const unsigned ad = vbase + (lane & 1) * 256 + (lane >> 4) * 16;
#pragma unroll
  for (int s = 0; s < 4; ++s) a[s] = lds_ld<bf16x8>(ad + 64 * s);
}
DI void gemv_publish(rsrc_t rs, const Tag& tg, const f32x4v& d, unsigned base, unsigned bstride, int w, int lane) {
  if (lane < 32) {
    const float v = lane < 16 ? d[0] : d[1];
    st8(rs, base + ((lane >> 4) * bstride + 16 * w + (lane & 15)) * 8, g_lo(v, tg), tg.hi);
  }
}
DI unsigned gather4(rsrc_t rs, const Tag& tg, unsigned off, unsigned pstride) {
  v4u v[4];
#pragma unroll
  for (int pj = 0; pj < 4; ++pj) v[pj] = ld16(rs, off + pj * pstride);
  unsigned spins = 0;
  for (;;) {
    bool bad[4], any = false;
#pragma unroll
    for (int pj = 0; pj < 4; ++pj) { bad[pj] = !__all(g_ok2(v[pj], tg)); any = any || bad[pj]; }
    if (!any || ++spins >= SPIN_CAP) break;
    asm volatile("" ::: "memory");
#pragma unroll
    for (int pj = 0; pj < 4; ++pj) if (bad[pj]) v[pj] = ld16(rs, off + pj * pstride);
  }
  return pk_bf((g_val(v[0].x) + g_val(v[1].x)) + (g_val(v[2].x) + g_val(v[3].x)), (g_val(v[0].z) + g_val(v[1].z)) + (g_val(v[2].z) + g_val(v[3].z)));
}
DI Tag make_tag(u64 E) {
  const u64 T = E + 1;
  Tag tg;
  tg.hi = __builtin_amdgcn_readfirstlane((unsigned)(T >> 8));
  tg.lo8 = __builtin_amdgcn_readfirstlane((unsigned)T & 0xffu);
  tg.t32 = __builtin_amdgcn_readfirstlane((unsigned)T);
  tg.top = __builtin_amdgcn_readfirstlane((unsigned)(T >> 32));
  tg.nz = tg.t32 | 1u;
  return tg;
}

DI v4u poll_pair(rsrc_t rs, unsigned off, const Tag& tg) {
  v4u v = ld16(rs, off);
  unsigned spins = 0;
  while (!__all(g_ok2(v, tg)) && ++spins < SPIN_CAP) { __builtin_amdgcn_s_sleep(1); asm volatile("" ::: "memory"); v = ld16(rs, off); }
  return v;
}

DI void final_reduce(const Params& p, rsrc_t rs, const Tag& tg, int which, int a, int lane) {
  const int b = a >> 6, n0 = (a & 63) * 8, head = lane >> 1, hf = lane & 1;
  const unsigned off = WS_Y + ((((unsigned)which * 32 + head) * 2 + b) * 512 + n0 + 4 * hf) * 8;
  v4u v0 = ld16(rs, off), v1 = ld16(rs, off + 16);
  unsigned spins = 0;
  while (!(g_ok2(v0, tg) && g_ok2(v1, tg)) && ++spins < SPIN_CAP) { __builtin_amdgcn_s_sleep(1); asm volatile("" ::: "memory"); v0 = ld16(rs, off); v1 = ld16(rs, off + 16); }
  float r0 = swap32_add(g_val(v0.x), g_val(v1.x)), r1 = swap32_add(g_val(v0.z), g_val(v1.z));
  float x = swap16_add(r0, r1);
  x += dpp<0x4E>(x);
  x += dpp<0x124>(x);
  x += dpp<0x128>(x);
  if ((lane & 15) < 2) p.out[b * 1024 + which * 512 + n0 + 4 * hf + (lane >> 4)] = (u16)f2bf_bits(x);
}

DI float score4(const v4u& k, const v4u& q) {
  float d = dot2(k.x, q.x, 0.f); d = dot2(k.y, q.y, d); d = dot2(k.z, q.z, d); d = dot2(k.w, q.w, d);
  return rbf(rbf(row16_sum(d)) * SCALE);
}
template <int N>
DI void pv_record(rsrc_t rs, const Tag& tg, const float (&s)[N], const v4u (&V)[N], float m, unsigned ST, unsigned mg, unsigned rec, int tid) {
  const int lane = tid & 63, w = __builtin_amdgcn_readfirstlane(tid >> 6), c16 = lane & 15, g = lane >> 4;
  m = xrow_max(m);
  float l = 0.f, acc[8];
#pragma unroll
  for (int k = 0; k < 8; ++k) acc[k] = 0.f;
  const float mneg = -m * LOG2E;
#pragma unroll
  for (int i = 0; i < N; ++i) {
    const float pp = __builtin_amdgcn_exp2f(fmaf(s[i], LOG2E, mneg));
    l += pp;
#pragma unroll
    for (int k = 0; k < 4; ++k) { acc[2 * k] = fmaf(pp, bflo(V[i][k]), acc[2 * k]); acc[2 * k + 1] = fmaf(pp, bfhi(V[i][k]), acc[2 * k + 1]); }
  }
  l = xrow_sum(l);
  float r[4], t[2];
#pragma unroll
  for (int k = 0; k < 4; ++k) r[k] = swap32_add(acc[k], acc[k + 4]);
#pragma unroll
  for (int k = 0; k < 2; ++k) t[k] = swap16_add(r[k], r[k + 2]);
  lds_st<v2u>(mg + w * 512 + (8 * c16 + 4 * (g >> 1) + 2 * (g & 1)) * 4, v2u{__float_as_uint(t[0]), __float_as_uint(t[1])});
  if (lane == 0) lds_st<v2u>(ST + w * 8, v2u{__float_as_uint(m), __float_as_uint(l)});
  wg_barrier();
  if (tid < 192) {
    const f32x4 s0 = lds_ld<f32x4>(ST), s1 = lds_ld<f32x4>(ST + 16), s2 = lds_ld<f32x4>(ST + 32), s3 = lds_ld<f32x4>(ST + 48);
    const float M = fmaxf(fmaxf(fmaxf(s0[0], s0[2]), fmaxf(s1[0], s1[2])), fmaxf(fmaxf(s2[0], s2[2]), fmaxf(s3[0], s3[2])));
    float e[8];
    e[0] = __builtin_amdgcn_exp2f((s0[0] - M) * LOG2E); e[1] = __builtin_amdgcn_exp2f((s0[2] - M) * LOG2E);
    e[2] = __builtin_amdgcn_exp2f((s1[0] - M) * LOG2E); e[3] = __builtin_amdgcn_exp2f((s1[2] - M) * LOG2E);
    e[4] = __builtin_amdgcn_exp2f((s2[0] - M) * LOG2E); e[5] = __builtin_amdgcn_exp2f((s2[2] - M) * LOG2E);
    e[6] = __builtin_amdgcn_exp2f((s3[0] - M) * LOG2E); e[7] = __builtin_amdgcn_exp2f((s3[2] - M) * LOG2E);
    if (tid < 128) {
      float o = 0.f;
#pragma unroll
      for (int ww = 0; ww < 8; ++ww) o = fmaf(e[ww], lds_ld<float>(mg + ww * 512 + tid * 4), o);
      st8(rs, rec + tid * 8, g_lo(o, tg), tg.hi);
    } else if (tid == 128) {
      const float L = e[0] * s0[1] + e[1] * s0[3] + e[2] * s1[1] + e[3] * s1[3] + e[4] * s2[1] + e[5] * s2[3] + e[6] * s3[1] + e[7] * s3[3];
      st16(rs, rec + 1024, v4u{g_lo(L, tg), tg.hi, g_lo(M, tg), tg.hi});
    }
  }
}

DI void stream_wg(const Params& p, rsrc_t rs, const u64 E, int h, int j, unsigned lb) {
  const int b = j >> 1, half = j & 1, bh = b * 32 + h;
  const int tid = threadIdx.x, lane = tid & 63, w = __builtin_amdgcn_readfirstlane(tid >> 6), c16 = lane & 15, g = lane >> 4;
  const size_t off = ((size_t)bh * 8192 + 6144 + half * 1024 + w * (4 * NY)) * 128;
  const rsrc_t rk = mk_rsrc(p.kc + off, NY * 1024), rv = mk_rsrc(p.vc + off, NY * 1024);
  dma_slice(p.wqB + (size_t)(128 * j) * 4096 + h * 128, 4096, lb + 32768, w, lane);
  bf16x8 xa[4];
#pragma unroll
  for (int s = 0; s < 4; ++s) xa[s] = *reinterpret_cast<const bf16x8*>(p.xB + (lane & 1) * 512 + 128 * j + 32 * s + 8 * g);
  v4u K[NY], V[NY];
#pragma unroll
  for (int i = 0; i < KINIT; ++i) K[i] = ldnt_b(rk, lane * 16, i * 1024);
  asm volatile("s_waitcnt vmcnt(%0)" :: "i"(KINIT) : "memory");
  wg_barrier();
  const Tag tg = make_tag(E);
  {
    const f32x4v d = gemv_mfma(xa, lb + 32768, w, lane);
    gemv_publish(rs, tg, d, WS_Q1 + (((unsigned)h * 4 + j) * 2 + 1) * 2048, 128, w, lane);
  }
  STAMP(1);
  v4u qa[4];
  const unsigned qoff = WS_Q1 + ((((unsigned)h * 4) * 2 + 1) * 2 + b) * 1024 + lane * 16;
#pragma unroll
  for (int i = KINIT; i < 2 * NY; ++i) {
    if (i < NY) K[i] = ldnt_b(rk, lane * 16, i * 1024); else V[i - NY] = ldnt_b(rv, lane * 16, (i - NY) * 1024);
    if (i == NY - 4) { if (w == 0) {
#pragma unroll
        for (int pj = 0; pj < 4; ++pj) qa[pj] = ld16(rs, qoff + pj * 4096); } }
    if (i == NY + 4) {
      if (w == 0) {
        unsigned spins = 0;
        while (!__all(g_ok2(qa[0], tg) && g_ok2(qa[1], tg) && g_ok2(qa[2], tg) && g_ok2(qa[3], tg)) && ++spins < SPIN_CAP) {
          asm volatile("" ::: "memory");
#pragma unroll
          for (int pj = 0; pj < 4; ++pj) qa[pj] = ld16(rs, qoff + pj * 4096);
        }
        const float q0 = (g_val(qa[0].x) + g_val(qa[1].x)) + (g_val(qa[2].x) + g_val(qa[3].x));
        const float q1 = (g_val(qa[0].z) + g_val(qa[1].z)) + (g_val(qa[2].z) + g_val(qa[3].z));
        lds_st<unsigned>(lb + lane * 4, pk_bf(q0, q1));
      }
    }
    if (i < KINIT + KSLOW) asm volatile("s_waitcnt vmcnt(%0)" :: "i"(SWIN) : "memory");
    else if (i >= KWIN) asm volatile("s_waitcnt vmcnt(%0)" :: "i"(KWIN) : "memory");
  }
  dma_slice(p.wuv + (size_t)(128 * j) * 4096 + h * 128, 4096, lb + 65536, w, lane);
  wg_barrier();
  STAMP(2);
  {
    const v4u q = lds_ld<v4u>(lb + c16 * 16);
    float s[NY], m = -INFINITY;
#pragma unroll
    for (int i = 0; i < NY; ++i) { s[i] = score4(K[i], q); m = fmaxf(m, s[i]); }
    dma_slice(p.woA + (size_t)(h * 128) * 512 + 128 * j, 512, lb + 98304, w, lane);
    STAMP(3);
    pv_record<NY>(rs, tg, s, V, m, lb + 512, lb + 1024, WS_REC + (((unsigned)h * 4 + j) * 2 + 0) * 1088, tid);
  }
  STAMP(4);
  const unsigned MLS = lb + 6144, V16 = lb + 7168, R2 = lb + 8192;
  if (tid < 128) {
    const v4u v = poll_pair(rs, WS_ML + ((unsigned)h * 128 + tid) * 16, tg);
    lds_st<v2u>(MLS + tid * 8, v2u{v.x << 8, v.z << 8});
  }
  wg_barrier();
  STAMP(5);
  {
    v4u pv[4];
#pragma unroll
    for (int i = 0; i < 4; ++i) pv[i] = ld16(rs, WS_PART + (((unsigned)(16 * w + 4 * i + g) * 32 + h) * 512 + 128 * j + 8 * c16) * 2);
    const int bb = w >> 2;
    const v2u ml = lds_ld<v2u>(MLS + (bb * 64 + lane) * 8);
    const float m = __uint_as_float(ml.x), l = __uint_as_float(ml.y);
    float M = m;
    M = fmaxf(M, dpp<0xB1>(M)); M = fmaxf(M, dpp<0x4E>(M)); M = fmaxf(M, dpp<0x141>(M)); M = fmaxf(M, dpp<0x140>(M)); M = xrow_max(M);
    const float e = __builtin_amdgcn_exp2f((m - M) * LOG2E);
    const float L = xrow_sum(row16_sum(e * l));
    const float wt = e / L;
    float wi[4];
#pragma unroll
    for (int i = 0; i < 4; ++i) wi[i] = __shfl(wt, (16 * w + 4 * i + g) & 63);
    float acc[8];
#pragma unroll
    for (int k = 0; k < 8; ++k) acc[k] = 0.f;
#pragma unroll
    for (int i = 0; i < 4; ++i) {
#pragma unroll
      for (int k = 0; k < 4; ++k) { acc[2 * k] = fmaf(wi[i], bflo(pv[i][k]), acc[2 * k]); acc[2 * k + 1] = fmaf(wi[i], bfhi(pv[i][k]), acc[2 * k + 1]); }
    }
    float r[4], t[2];
#pragma unroll
    for (int k = 0; k < 4; ++k) r[k] = swap32_add(acc[k], acc[k + 4]);
#pragma unroll
    for (int k = 0; k < 2; ++k) t[k] = swap16_add(r[k], r[k + 2]);
    lds_st<v2u>(R2 + (w * 128 + 8 * c16 + 4 * (g >> 1) + 2 * (g & 1)) * 4, v2u{__float_as_uint(t[0]), __float_as_uint(t[1])});
  }
  wg_barrier();
  if (tid < 128) {
    const int bb = tid >> 6, cp = tid & 63;
    float x0 = 0.f, x1 = 0.f;
#pragma unroll
    for (int ww = 0; ww < 4; ++ww) { const v2u t = lds_ld<v2u>(R2 + ((4 * bb + ww) * 128 + 2 * cp) * 4); x0 += __uint_as_float(t.x); x1 += __uint_as_float(t.y); }
    lds_st<unsigned>(V16 + tid * 4, pk_bf(x0, x1));
  }
  wg_barrier();
  {
    bf16x8 f[4];
    vec_frags(f, V16, lane);
    const f32x4v d = gemv_mfma(f, lb + 65536, w, lane);
    gemv_publish(rs, tg, d, WS_C4 + ((unsigned)h * 4 + j) * 2048, 128, w, lane);
  }
  STAMP(6);
  if (tid < 128) {
    const unsigned v = gather4(rs, tg, WS_C4 + ((unsigned)h * 4) * 2048 + (tid >> 6) * 1024 + (tid & 63) * 16, 2048);
    lds_st<unsigned>(V16 + 512 + tid * 4, v);
  }
  wg_barrier();
  {
    bf16x8 f[4];
    vec_frags(f, V16 + 512, lane);
    const f32x4v d = gemv_mfma(f, lb + 98304, w, lane);
    gemv_publish(rs, tg, d, WS_Y + (((unsigned)0 * 32 + h) * 1024 + 128 * j) * 8, 512, w, lane);
  }
  STAMP(7);
  if (w == 0) final_reduce(p, rs, tg, 0, h * 4 + j, lane);
  STAMP(8);
}

DI void chain_wg(const Params& p, rsrc_t rs, const u64 E, int a, unsigned lb) {
  const int h = a >> 2, j = a & 3;
  const int tid = threadIdx.x, lane = tid & 63, w = __builtin_amdgcn_readfirstlane(tid >> 6), g = lane >> 4, c16 = lane & 15;
  const unsigned scr = lb + SCR;
  dma_slice(p.wqA + (size_t)(128 * j) * 4096 + h * 128, 4096, scr, w, lane);
  bf16x8 fa[4], WK[4];
#pragma unroll
  for (int s = 0; s < 4; ++s) fa[s] = *reinterpret_cast<const bf16x8*>(p.xA + (lane & 1) * 512 + 128 * j + 32 * s + 8 * g);
  {
    const rsrc_t rw = mk_rsrc(p.wuk + (size_t)(128 * j + 16 * w) * 4096 + h * 128, 16 * 8192);
#pragma unroll
    for (int s = 0; s < 4; ++s) WK[s] = __builtin_bit_cast(bf16x8, ldnt_b(rw, c16 * 8192 + g * 16, 64 * s));
  }
  const int bA = a >> 6;
  asm volatile("s_barrier" ::: "memory");
  dma_rows1k<0, CK0>(p.ckv + (size_t)a * 128 * 512, lb, w, lane);
  asm volatile("s_waitcnt vmcnt(%0)" :: "i"(4 + CK0) : "memory");
  wg_barrier();
  const Tag tg = make_tag(E);
  {
    const f32x4v d = gemv_mfma(fa, scr, w, lane);
    gemv_publish(rs, tg, d, WS_Q1 + (((unsigned)h * 4 + j) * 2 + 0) * 2048, 128, w, lane);
  }
  STAMP(1);
  dma_rows1k<CK0, CK1>(p.ckv + (size_t)a * 128 * 512, lb, w, lane);
  wg_barrier();
  if (tid < 128) {
    const unsigned v = gather4(rs, tg, WS_Q1 + (((unsigned)h * 4) * 4 + (tid >> 6)) * 1024 + (tid & 63) * 16, 4096);
    lds_st<unsigned>(scr + tid * 4, v);
  } else if (tid < 192) {
    const unsigned v = gather4(rs, tg, WS_Q1 + (((unsigned)h * 4) * 4 + 2 + (j >> 1)) * 1024 + lane * 16, 4096);
    lds_st<unsigned>(scr + 512 + lane * 4, v);
  }
  wg_barrier();
  STAMP(2);
  const v4u qB = lds_ld<v4u>(scr + 512 + c16 * 16);
  {
    bf16x8 qf[4];
    vec_frags(qf, scr, lane);
    f32x4v d = {0.f, 0.f, 0.f, 0.f};
#pragma unroll
    for (int s = 0; s < 4; ++s) d = __builtin_amdgcn_mfma_f32_16x16x32_bf16(WK[s], qf[s], d, 0, 0, 0);
    if (c16 < 2)
      st16(rs, WS_QL + (((unsigned)c16 * 32 + h) * 256 + 64 * j + 8 * w + 2 * g) * 8, v4u{pk_bf(d[0], d[1]), tg.t32, pk_bf(d[2], d[3]), tg.t32});
  }
  STAMP(3);
  dma_rows1k<CK1, 16>(p.ckv + (size_t)a * 128 * 512, lb, w, lane);
  const int r32 = lane & 31, hh = lane >> 5;
  float sc[8];
  s16x4 xt[32];
  bf16x8 ka[16];
  const unsigned fr = swz(r32);
  {
    v4u v[8];
    const unsigned base = WS_QL + (unsigned)bA * 65536 + tid * 16;
#pragma unroll
    for (int i = 0; i < 8; ++i) { v[i] = ld16(rs, base + i * 8192); }
    asm volatile("s_waitcnt vmcnt(8)" ::: "memory");
    wg_barrier();
    STAMP(11);
    {
      const unsigned abase = lb + (32 * (w & 3) + r32) * 1024 + 512 * (w >> 2);
#pragma unroll
      for (int s = 0; s < 16; ++s) ka[s] = lds_ld<bf16x8>(abase + 16 * ((unsigned)((2 * s + hh) & 15) ^ fr) + 256 * (s >> 3));
      const unsigned blk = (lane >> 4) & 1, q = (lane & 15) >> 2, pp = lane & 3;
      unsigned tb[4];
#pragma unroll
      for (int mt = 0; mt < 2; ++mt)
#pragma unroll
        for (int t = 0; t < 2; ++t) {
          const unsigned row = 8 * hh + 4 * t + q, chn = 8 * w + 4 * mt + 2 * blk + (pp >> 1);
          tb[mt * 2 + t] = lb + row * 1024 + 16 * (chn ^ swz(row)) + 8 * (pp & 1);
        }
#pragma unroll
      for (int hf = 0; hf < 2; ++hf) {
        asm volatile(
            "ds_read_b64_tr_b16 %0, %16\n\tds_read_b64_tr_b16 %1, %17\n\tds_read_b64_tr_b16 %2, %18\n\tds_read_b64_tr_b16 %3, %19\n\t"
            "ds_read_b64_tr_b16 %4, %16 offset:16384\n\tds_read_b64_tr_b16 %5, %17 offset:16384\n\tds_read_b64_tr_b16 %6, %18 offset:16384\n\tds_read_b64_tr_b16 %7, %19 offset:16384\n\t"
            "ds_read_b64_tr_b16 %8, %16 offset:32768\n\tds_read_b64_tr_b16 %9, %17 offset:32768\n\tds_read_b64_tr_b16 %10, %18 offset:32768\n\tds_read_b64_tr_b16 %11, %19 offset:32768\n\t"
            "ds_read_b64_tr_b16 %12, %16 offset:49152\n\tds_read_b64_tr_b16 %13, %17 offset:49152\n\tds_read_b64_tr_b16 %14, %18 offset:49152\n\tds_read_b64_tr_b16 %15, %19 offset:49152\n\t"
            "s_waitcnt lgkmcnt(0)"
            : "=&v"(xt[16 * hf + 0]), "=&v"(xt[16 * hf + 1]), "=&v"(xt[16 * hf + 2]), "=&v"(xt[16 * hf + 3]),
              "=&v"(xt[16 * hf + 4]), "=&v"(xt[16 * hf + 5]), "=&v"(xt[16 * hf + 6]), "=&v"(xt[16 * hf + 7]),
              "=&v"(xt[16 * hf + 8]), "=&v"(xt[16 * hf + 9]), "=&v"(xt[16 * hf + 10]), "=&v"(xt[16 * hf + 11]),
              "=&v"(xt[16 * hf + 12]), "=&v"(xt[16 * hf + 13]), "=&v"(xt[16 * hf + 14]), "=&v"(xt[16 * hf + 15])
            : "v"(tb[0] + hf * 65536), "v"(tb[1] + hf * 65536), "v"(tb[2] + hf * 65536), "v"(tb[3] + hf * 65536) : "memory");
      }
    }
    STAMP(12);
    unsigned spins = 0;
    const unsigned cq = tid & 127, h0 = tid >> 7;
    for (;;) {
      bool bad[8], any = false;
#pragma unroll
      for (int i = 0; i < 8; ++i) { bad[i] = !__all(v[i].y == tg.t32 && v[i].w == tg.t32); any = any || bad[i]; }
      if (!any || ++spins >= SPIN_CAP) break;
      asm volatile("" ::: "memory");
#pragma unroll
      for (int i = 0; i < 8; ++i) if (bad[i]) v[i] = ld16(rs, base + i * 8192);
    }
#pragma unroll
    for (int i = 0; i < 8; ++i) {
      const unsigned head = 4 * i + h0;
      lds_st<v2u>(scr + head * 1024 + 16 * ((cq >> 1) ^ swz(head)) + 8 * (cq & 1), v2u{v[i].x, v[i].z});
    }
  }
  wg_barrier();
  STAMP(4);
  const size_t koff = ((size_t)((j >> 1) * 32 + h) * 8192 + 6144 + (j & 1) * 1024 + 32 * NY + w * (4 * NX)) * 128;
  const rsrc_t rk = mk_rsrc(p.kc + koff, NX * 1024), rv = mk_rsrc(p.vc + koff, NX * 1024);
  v4u K[NX], V[NX];
#pragma unroll
  for (int i = 0; i < 7; ++i) K[i] = ldnt_b(rk, lane * 16, i * 1024);
  {
    const int ch = w >> 2;
    f32x16 acc;
#pragma unroll
    for (int i = 0; i < 16; ++i) acc[i] = 0.f;
    const unsigned bbase = scr + r32 * 1024 + 512 * ch;
#pragma unroll
    for (int s = 0; s < 16; ++s) {
      const bf16x8 bv = lds_ld<bf16x8>(bbase + 16 * ((unsigned)((2 * s + hh) & 15) ^ fr) + 256 * (s >> 3));
      acc = __builtin_amdgcn_mfma_f32_32x32x16_bf16(ka[s], bv, acc, 0, 0, 0);
      if ((s & 7) == 7) __builtin_amdgcn_sched_barrier(0);
    }
    f32x4 s0, s1;
#pragma unroll
    for (int k = 0; k < 4; ++k) { s0[k] = ch ? acc[k] : acc[8 + k]; s1[k] = ch ? acc[4 + k] : acc[12 + k]; }
    const unsigned XB = lb + 32768;
    lds_st<f32x4>(XB + w * 2048 + lane * 16, s0);
    lds_st<f32x4>(XB + w * 2048 + 1024 + lane * 16, s1);
    wg_barrier();
    const f32x4 t0 = lds_ld<f32x4>(XB + (w ^ 4) * 2048 + lane * 16), t1 = lds_ld<f32x4>(XB + (w ^ 4) * 2048 + 1024 + lane * 16);
#pragma unroll
    for (int k = 0; k < 4; ++k) { sc[k] = ((ch ? acc[8 + k] : acc[k]) + t0[k]) * SCALE; sc[4 + k] = ((ch ? acc[12 + k] : acc[4 + k]) + t1[k]) * SCALE; }
  }
#pragma unroll
  for (int i = 7; i < NX; ++i) K[i] = ldnt_b(rk, lane * 16, i * 1024);
  const unsigned MX = scr + 16384, LS = scr + 17408, PB = scr + 18432;
  float Mh;
  {
    float m = sc[0];
#pragma unroll
    for (int k = 1; k < 8; ++k) m = fmaxf(m, sc[k]);
    { auto t = __builtin_amdgcn_permlane32_swap(__float_as_uint(m), __float_as_uint(m), false, false); m = fmaxf(__uint_as_float(t[0]), __uint_as_float(t[1])); }
    if (lane < 32) lds_st<float>(MX + (lane * 8 + w) * 4, m);
    wg_barrier();
    const f32x4 ma = lds_ld<f32x4>(MX + r32 * 32), mb = lds_ld<f32x4>(MX + r32 * 32 + 16);
    float M = fmaxf(fmaxf(fmaxf(ma[0], ma[1]), fmaxf(ma[2], ma[3])), fmaxf(fmaxf(mb[0], mb[1]), fmaxf(mb[2], mb[3])));
    M = round24(M);
    Mh = M;
    const float mneg = -M * LOG2E;
    unsigned pk[4]; float l = 0.f;
#pragma unroll
    for (int k = 0; k < 4; ++k) {
      const float p0 = __builtin_amdgcn_exp2f(fmaf(sc[2 * k], LOG2E, mneg)), p1 = __builtin_amdgcn_exp2f(fmaf(sc[2 * k + 1], LOG2E, mneg));
      pk[k] = pk_bf(p0, p1);
      l += bflo(pk[k]) + bfhi(pk[k]);
    }
    { auto t = __builtin_amdgcn_permlane32_swap(__float_as_uint(l), __float_as_uint(l), false, false); l = __uint_as_float(t[0]) + __uint_as_float(t[1]); }
    if (lane < 32) lds_st<float>(LS + (lane * 8 + w) * 4, l);
    const int kt = w & 3, ch = w >> 2;
    lds_st<v2u>(PB + r32 * 272 + (32 * kt + 16 * ch + 4 * hh) * 2, v2u{pk[0], pk[1]});
    lds_st<v2u>(PB + r32 * 272 + (32 * kt + 16 * ch + 8 + 4 * hh) * 2, v2u{pk[2], pk[3]});
  }
  wg_barrier();
  f32x16 o0, o1;
#pragma unroll
  for (int i = 0; i < 16; ++i) { o0[i] = 0.f; o1[i] = 0.f; }
#pragma unroll
  for (int s = 0; s < 8; ++s) {
    const bf16x8 pv = lds_ld<bf16x8>(PB + r32 * 272 + 32 * s + 16 * hh);
    o0 = __builtin_amdgcn_mfma_f32_32x32x16_bf16(__builtin_shufflevector(xt[4 * s], xt[4 * s + 1], 0, 1, 2, 3, 4, 5, 6, 7), pv, o0, 0, 0, 0);
    o1 = __builtin_amdgcn_mfma_f32_32x32x16_bf16(__builtin_shufflevector(xt[4 * s + 2], xt[4 * s + 3], 0, 1, 2, 3, 4, 5, 6, 7), pv, o1, 0, 0, 0);
  }
  STAMP(5);
  {
    const unsigned tb = lb + 32768 + w * 4096;
#pragma unroll
    for (int gq = 0; gq < 4; ++gq) {
      lds_st<v2u>(tb + r32 * 128 + 16 * (gq ^ (r32 & 7)) + 8 * hh, v2u{pk_bf(o0[4 * gq], o0[4 * gq + 1]), pk_bf(o0[4 * gq + 2], o0[4 * gq + 3])});
      lds_st<v2u>(tb + r32 * 128 + 16 * ((4 + gq) ^ (r32 & 7)) + 8 * hh, v2u{pk_bf(o1[4 * gq], o1[4 * gq + 1]), pk_bf(o1[4 * gq + 2], o1[4 * gq + 3])});
    }
    asm volatile("s_waitcnt lgkmcnt(0)" ::: "memory");
#pragma unroll
    for (int i = 0; i < 4; ++i) {
      const int head = 8 * i + (lane >> 3);
      const v4u v = lds_ld<v4u>(tb + head * 128 + 16 * ((lane & 7) ^ (head & 7)));
      st16(rs, WS_PART + (((unsigned)a * 32 + head) * 512 + 64 * w + 8 * (lane & 7)) * 2, v);
    }
    asm volatile("" ::: "memory");
#pragma unroll
    for (int i = 0; i < 6; ++i) V[i] = ldnt_b(rv, lane * 16, i * 1024);
    asm volatile("s_waitcnt vmcnt(6)" ::: "memory");
  }
  wg_barrier();
  if (w == 0 && lane < 32) {
    const f32x4 la = lds_ld<f32x4>(LS + lane * 32), lc = lds_ld<f32x4>(LS + lane * 32 + 16);
    const float l = ((la[0] + la[1]) + (la[2] + la[3])) + ((lc[0] + lc[1]) + (lc[2] + lc[3]));
    st16(rs, WS_ML + ((unsigned)lane * 128 + a) * 16, v4u{g_lo(Mh, tg), tg.hi, g_lo(l, tg), tg.hi});
  }
  STAMP(6);
  if (a == 0 && w == 7) {
    v4u d0 = ld16(rs, WS_Q1 + ((unsigned)lane * 2 + 1) * 2048), d1 = ld16(rs, WS_Q1 + ((unsigned)(lane + 64) * 2 + 1) * 2048);
    unsigned spins = 0;
    while (!__all(g_ok2(d0, tg) && g_ok2(d1, tg)) && ++spins < SPIN_CAP) {
      __builtin_amdgcn_s_sleep(2); asm volatile("" ::: "memory");
      d0 = ld16(rs, WS_Q1 + ((unsigned)lane * 2 + 1) * 2048); d1 = ld16(rs, WS_Q1 + ((unsigned)(lane + 64) * 2 + 1) * 2048);
    }
    if (lane == 0) __hip_atomic_store(reinterpret_cast<u64*>(p.ws + WS_EPOCH), ((u64)tg.top << 32) | tg.t32, __ATOMIC_RELAXED, __HIP_MEMORY_SCOPE_AGENT);
  }
#pragma unroll
  for (int i = 6; i < NX; ++i) V[i] = ldnt_b(rv, lane * 16, i * 1024);
  {
    float s[NX], m = -INFINITY;
#pragma unroll
    for (int i = 0; i < NX; ++i) { s[i] = score4(K[i], qB); m = fmaxf(m, s[i]); }
    dma_slice(p.woB + (size_t)(h * 128) * 512 + 128 * j, 512, lb, w, lane);
    STAMP(7);
    pv_record<NX>(rs, tg, s, V, m, lb + 65536, lb + 66560, WS_REC + (((unsigned)h * 4 + j) * 2 + 1) * 1088, tid);
  }
  STAMP(8);
  const unsigned V16 = lb + 71680;
  if (tid < 128) {
    const int bb = tid >> 6, dp = tid & 63;
    const unsigned r0 = WS_REC + (((unsigned)h * 4 + 2 * bb) * 2) * 1088;
    v4u o[4], st[4];
#pragma unroll
    for (int r = 0; r < 4; ++r) { o[r] = ld16(rs, r0 + r * 1088 + dp * 16); st[r] = ld16(rs, r0 + r * 1088 + 1024); }
    unsigned spins = 0;
    for (;;) {
      bool bad[4], any = false;
#pragma unroll
      for (int r = 0; r < 4; ++r) { bad[r] = !__all(g_ok2(o[r], tg) && g_ok2(st[r], tg)); any = any || bad[r]; }
      if (!any || ++spins >= SPIN_CAP) break;
      asm volatile("" ::: "memory");
#pragma unroll
      for (int r = 0; r < 4; ++r) if (bad[r]) { o[r] = ld16(rs, r0 + r * 1088 + dp * 16); st[r] = ld16(rs, r0 + r * 1088 + 1024); }
    }
    const float Mx = fmaxf(fmaxf(g_val(st[0].z), g_val(st[1].z)), fmaxf(g_val(st[2].z), g_val(st[3].z)));
    float L = 0.f, o0 = 0.f, o1 = 0.f;
#pragma unroll
    for (int r = 0; r < 4; ++r) {
      const float e = __builtin_amdgcn_exp2f((g_val(st[r].z) - Mx) * LOG2E);
      L = fmaf(e, g_val(st[r].x), L); o0 = fmaf(e, g_val(o[r].x), o0); o1 = fmaf(e, g_val(o[r].z), o1);
    }
    const float inv = 1.f / L;
    lds_st<unsigned>(V16 + tid * 4, pk_bf(o0 * inv, o1 * inv));
  }
  asm volatile("s_waitcnt vmcnt(0)" ::: "memory");
  wg_barrier();
  STAMP(9);
  {
    bf16x8 f[4];
    vec_frags(f, V16, lane);
    const f32x4v d = gemv_mfma(f, lb, w, lane);
    gemv_publish(rs, tg, d, WS_Y + (((unsigned)1 * 32 + h) * 1024 + 128 * j) * 8, 512, w, lane);
  }
  if (w == 0) final_reduce(p, rs, tg, 1, a, lane);
  STAMP(10);
}

__global__ void __launch_bounds__(512, 2) fused_decode_kernel(Params p) {
  extern __shared__ __attribute__((aligned(16))) char smem[];
  const unsigned lb = (unsigned)(uintptr_t)(LAS char*)smem;
  STAMP(0);
  u64 pc; asm volatile("s_getpc_b64 %0" : "=s"(pc));
  const rsrc_t rs = __builtin_amdgcn_make_buffer_rsrc(p.ws, 0, WS_END, 0x00020000);
  const u64 E = __hip_atomic_load(reinterpret_cast<u64*>(p.ws + WS_EPOCH), __ATOMIC_RELAXED, __HIP_MEMORY_SCOPE_AGENT);
  const int gidx = blockIdx.x;
  const int role = (gidx >> 3) & 1, k = gidx >> 4, head = 4 * (gidx & 7) + (k >> 2), j = k & 3;
  if (role == 0) {
    const unsigned o = (threadIdx.x * 64u < CODE_BYTES) ? threadIdx.x * 64u : 0u;
    const u64 ad = (pc & ~63ull) + o;
    unsigned keep;
    asm volatile("s_mov_b32 %0, m0\n\ts_mov_b32 m0, %2\n\ts_nop 0\n\tglobal_load_lds_dword %1, off\n\ts_mov_b32 m0, %0"
                 : "=&s"(keep) : "v"(ad), "s"(lb + 16384u + (unsigned)__builtin_amdgcn_readfirstlane(threadIdx.x >> 6) * 256u) : "memory");
    stream_wg(p, rs, E, head, j, lb);
  }
  else chain_wg(p, rs, E, head * 4 + j, lb);
}

extern "C" void kernel_launch(void* const* d_in, const int* in_sizes, int n_in, void* d_out, int out_size, void* d_ws, size_t ws_size,
                              hipStream_t stream) {
  Params p;
  p.xA = (const u16*)d_in[0]; p.ckv = (const u16*)d_in[1]; p.wqA = (const u16*)d_in[2]; p.wuk = (const u16*)d_in[3];
  p.wuv = (const u16*)d_in[4]; p.woA = (const u16*)d_in[5]; p.xB = (const u16*)d_in[6]; p.kc = (const u16*)d_in[7];
  p.vc = (const u16*)d_in[8]; p.wqB = (const u16*)d_in[9]; p.woB = (const u16*)d_in[10];
  p.out = (u16*)d_out; p.ws = (unsigned char*)d_ws;
  (void)hipFuncSetAttribute((const void*)fused_decode_kernel, hipFuncAttributeMaxDynamicSharedMemorySize, LDS_BYTES);
  fused_decode_kernel<<<256, 512, LDS_BYTES, stream>>>(p);
}
```

```cpp
#define XLIM 3100
#include <hip/hip_runtime.h>
#include <stdint.h>
#define SHPOL " nt"

typedef unsigned short u16;
typedef unsigned long long u64;
typedef unsigned v4u __attribute__((ext_vector_type(4)));
typedef unsigned v2u __attribute__((ext_vector_type(2)));
typedef float f32x4 __attribute__((ext_vector_type(4)));
typedef float f32x16 __attribute__((ext_vector_type(16)));
typedef short bf16x8 __attribute__((ext_vector_type(8)));
typedef short s16x4 __attribute__((ext_vector_type(4)));
typedef __bf16 bf16x2 __attribute__((ext_vector_type(2)));
#define LAS __attribute__((address_space(3)))
#define DI __device__ __forceinline__

constexpr unsigned WS_EPOCH = 0;
constexpr unsigned WS_Q1 = 4096;
constexpr unsigned WS_QL = WS_Q1 + 65536u * 8;
constexpr unsigned WS_ML = WS_QL + 16384u * 8;
constexpr unsigned WS_C4 = WS_ML + 8192u * 8;
constexpr unsigned WS_REC = WS_C4 + 32768u * 8;
constexpr unsigned WS_Y = WS_REC + 32u * 8 * 136 * 8;
constexpr unsigned WS_PART = WS_Y + 65536u * 8;
constexpr unsigned WS_STAMP = WS_PART + 128u * 32 * 512 * 2;
constexpr unsigned WS_END = WS_STAMP + 256u * 16 * 8;

constexpr unsigned CODE_BYTES = 16384;
constexpr int LDS_BYTES = 163840;
constexpr unsigned SCR = 131072;
constexpr unsigned SPIN_CAP = 1u << 16;
#define KINIT_ 1
#define KWIN_ 8
#define CK1_ 12
#define VPRE_ 8
#define KSLOW_ 3
#define SWIN_ 0
#define CK0_ 6
constexpr int CK0 = CK0_;
#define NY_ 20
constexpr int NY = NY_, NX = 32 - NY_;
constexpr int KSLOW = KSLOW_, SWIN = SWIN_;
constexpr int KINIT = KINIT_, KWIN = KWIN_, CK1 = CK1_, VPRE = VPRE_;
constexpr float SCALE = 0.08838834764831845f;
constexpr float LOG2E = 1.4426950408889634f;

struct Params {
  const u16 *xA, *ckv, *wqA, *wuk, *wuv, *woA, *xB, *kc, *vc, *wqB, *woB;
  u16* out;
  unsigned char* ws;
};

DI float bflo(unsigned w) { return __uint_as_float(w << 16); }
DI float bfhi(unsigned w) { return __uint_as_float(w & 0xffff0000u); }
DI unsigned f2bf_bits(float f) { return (unsigned)__builtin_bit_cast(unsigned short, (__bf16)f); }
DI float rbf(float f) { return __uint_as_float(f2bf_bits(f) << 16); }
DI unsigned pk_bf(float lo, float hi) { return f2bf_bits(lo) | (f2bf_bits(hi) << 16); }
DI float dot2(unsigned a, unsigned b, float acc) {
  return __builtin_amdgcn_fdot2_f32_bf16(__builtin_bit_cast(bf16x2, a), __builtin_bit_cast(bf16x2, b), acc, false);
}
template <int CTRL> DI float dpp(float x) {
  return __builtin_bit_cast(float, __builtin_amdgcn_mov_dpp(__builtin_bit_cast(int, x), CTRL, 0xf, 0xf, true));
}
DI float row16_sum(float x) { x += dpp<0xB1>(x); x += dpp<0x4E>(x); x += dpp<0x141>(x); x += dpp<0x140>(x); return x; }
DI float xrow_max(float x) {
  auto s = __builtin_amdgcn_permlane16_swap(__float_as_uint(x), __float_as_uint(x), false, false);
  x = fmaxf(__uint_as_float(s[0]), __uint_as_float(s[1]));
  auto t = __builtin_amdgcn_permlane32_swap(__float_as_uint(x), __float_as_uint(x), false, false);
  return fmaxf(__uint_as_float(t[0]), __uint_as_float(t[1]));
}
DI float xrow_sum(float x) {
  auto s = __builtin_amdgcn_permlane16_swap(__float_as_uint(x), __float_as_uint(x), false, false);
  x = __uint_as_float(s[0]) + __uint_as_float(s[1]);
  auto t = __builtin_amdgcn_permlane32_swap(__float_as_uint(x), __float_as_uint(x), false, false);
  return __uint_as_float(t[0]) + __uint_as_float(t[1]);
}
DI float swap32_add(float a, float b) {
  auto t = __builtin_amdgcn_permlane32_swap(__float_as_uint(a), __float_as_uint(b), false, false);
  return __uint_as_float(t[0]) + __uint_as_float(t[1]);
}
DI float swap16_add(float a, float b) {
  auto t = __builtin_amdgcn_permlane16_swap(__float_as_uint(a), __float_as_uint(b), false, false);
  return __uint_as_float(t[0]) + __uint_as_float(t[1]);
}
DI v4u ldnt(const u16* p) { return __builtin_nontemporal_load(reinterpret_cast<const v4u*>(p)); }
DI void wg_barrier() { asm volatile("s_waitcnt lgkmcnt(0)\n\ts_barrier" ::: "memory"); }
DI void glds16_nt(const void* gsrc, unsigned lds_dst) {
  unsigned keep;
  asm volatile("s_mov_b32 %0, m0\n\ts_mov_b32 m0, %2\n\ts_nop 0\n\tglobal_load_lds_dwordx4 %1, off nt\n\ts_mov_b32 m0, %0"
               : "=&s"(keep) : "v"(gsrc), "s"(lds_dst) : "memory");
}
DI void glds16_def(const void* gsrc, unsigned lds_dst) {
  unsigned keep;
  asm volatile("s_mov_b32 %0, m0\n\ts_mov_b32 m0, %2\n\ts_nop 0\n\tglobal_load_lds_dwordx4 %1, off" SHPOL "\n\ts_mov_b32 m0, %0"
               : "=&s"(keep) : "v"(gsrc), "s"(lds_dst) : "memory");
}
typedef __amdgpu_buffer_rsrc_t rsrc_t;
DI rsrc_t mk_rsrc(const void* base, unsigned bytes) {
  const u64 b = (u64)base;
  const unsigned lo = __builtin_amdgcn_readfirstlane((unsigned)b), hi = __builtin_amdgcn_readfirstlane((unsigned)(b >> 32));
  return __builtin_amdgcn_make_buffer_rsrc((void*)(((u64)hi << 32) | lo), 0, bytes, 0x00020000);
}
DI v4u ldnt_b(rsrc_t r, unsigned voff, unsigned soff) { return __builtin_amdgcn_raw_buffer_load_b128(r, voff, soff, 2); }
DI v4u ld16(rsrc_t r, unsigned off) { return __builtin_amdgcn_raw_buffer_load_b128(r, off, 0, 16); }
DI void st8(rsrc_t r, unsigned off, unsigned lo, unsigned hi) { v2u v = {lo, hi}; __builtin_amdgcn_raw_buffer_store_b64(v, r, off, 0, 16); }
DI void st16(rsrc_t r, unsigned off, v4u v) { __builtin_amdgcn_raw_buffer_store_b128(v, r, off, 0, 16); }

struct Tag { unsigned hi, lo8, t32, top, nz; };
DI unsigned g_lo(float v, const Tag& t) { return ((__float_as_uint(v) + 0x80u) >> 8) | (t.lo8 << 24); }
DI float g_val(unsigned lo) { return __uint_as_float(lo << 8); }
DI bool g_ok(unsigned lo, unsigned hi, const Tag& t) { return hi == t.hi && (lo >> 24) == t.lo8; }
DI bool g_ok2(const v4u& v, const Tag& t) { return g_ok(v.x, v.y, t) && g_ok(v.z, v.w, t); }
DI float round24(float v) { return __uint_as_float((__float_as_uint(v) + 0x80u) & 0xffffff00u); }
DI unsigned swz(unsigned r) { return ((r & 3u) << 2) | ((r >> 2) & 3u); }

#define STAMP(k) do { } while (0)

template <typename T> DI void lds_st(unsigned addr, T v) { *(LAS T*)(uintptr_t)addr = v; }
template <typename T> DI T lds_ld(unsigned addr) { return *(LAS T*)(uintptr_t)addr; }

typedef float f32x4v __attribute__((ext_vector_type(4)));

DI void dma_slice(const u16* src0, size_t row_stride, unsigned dst, int w, int lane) {
  asm volatile("" : "+v"(lane));
  const int g = lane >> 4, c16 = lane & 15;
  const u16* s = src0 + (size_t)(16 * w + g) * row_stride;
#pragma unroll
  for (int i = 0; i < 4; ++i) glds16_nt(s + (size_t)(4 * i) * row_stride + 8 * (c16 ^ ((g << 2) | i)), dst + (16 * w + 4 * i) * 256);
}
DI f32x4v gemv_mfma(const bf16x8 (&a)[4], unsigned wbase, int w, int lane) {
  const unsigned g = lane >> 4, q = (lane & 15) >> 2, pp = lane & 3;
  const unsigned chn = 2 * w + (pp >> 1);
  const unsigned a0 = wbase + (8 * g + q) * 256 + 16 * (chn ^ ((q << 2) | ((2 * g) & 3))) + 8 * (pp & 1);
  const unsigned a1 = wbase + (8 * g + 4 + q) * 256 + 16 * (chn ^ ((q << 2) | ((2 * g + 1) & 3))) + 8 * (pp & 1);
  s16x4 x[8];
  asm volatile("ds_read_b64_tr_b16 %0, %8\n\tds_read_b64_tr_b16 %1, %9\n\t"
               "ds_read_b64_tr_b16 %2, %8 offset:8192\n\tds_read_b64_tr_b16 %3, %9 offset:8192\n\t"
               "ds_read_b64_tr_b16 %4, %8 offset:16384\n\tds_read_b64_tr_b16 %5, %9 offset:16384\n\t"
               "ds_read_b64_tr_b16 %6, %8 offset:24576\n\tds_read_b64_tr_b16 %7, %9 offset:24576\n\ts_waitcnt lgkmcnt(0)"
               : "=&v"(x[0]), "=&v"(x[1]), "=&v"(x[2]), "=&v"(x[3]), "=&v"(x[4]), "=&v"(x[5]), "=&v"(x[6]), "=&v"(x[7])
               : "v"(a0), "v"(a1) : "memory");
  f32x4v d = {0.f, 0.f, 0.f, 0.f};
#pragma unroll
  for (int s = 0; s < 4; ++s)
    d = __builtin_amdgcn_mfma_f32_16x16x32_bf16(a[s], __builtin_shufflevector(x[2 * s], x[2 * s + 1], 0, 1, 2, 3, 4, 5, 6, 7), d, 0, 0, 0);
  return d;
}

template <int I0, int I1, bool NT = true> DI void dma_rows1k(const u16* src, unsigned dst, int w, int lane) {
  asm volatile("" : "+v"(lane));
  const u16* s = src + (size_t)(16 * w) * 512;
#pragma unroll
  for (int i = I0; i < I1; ++i) {
    if (NT) glds16_nt(s + i * 512 + 8 * (lane ^ swz(i)), dst + (16 * w + i) * 1024);
    else glds16_def(s + i * 512 + 8 * (lane ^ swz(i)), dst + (16 * w + i) * 1024);
  }
}
DI void vec_frags(bf16x8 (&a)[4], unsigned vbase, int lane) {
  const unsigned ad = vbase + (lane & 1) * 256 + (lane >> 4) * 16;
#pragma unroll
  for (int s = 0; s < 4; ++s) a[s] = lds_ld<bf16x8>(ad + 64 * s);
}
DI void gemv_publish(rsrc_t rs, const Tag& tg, const f32x4v& d, unsigned base, unsigned bstride, int w, int lane) {
  if (lane < 32) {
    const float v = lane < 16 ? d[0] : d[1];
    st8(rs, base + ((lane >> 4) * bstride + 16 * w + (lane & 15)) * 8, g_lo(v, tg), tg.hi);
  }
}
DI unsigned gather4(rsrc_t rs, const Tag& tg, unsigned off, unsigned pstride) {
  v4u v[4];
#pragma unroll
  for (int pj = 0; pj < 4; ++pj) v[pj] = ld16(rs, off + pj * pstride);
  unsigned spins = 0;
  for (;;) {
    bool bad[4], any = false;
#pragma unroll
    for (int pj = 0; pj < 4; ++pj) { bad[pj] = !__all(g_ok2(v[pj], tg)); any = any || bad[pj]; }
    if (!any || ++spins >= SPIN_CAP) break;
    asm volatile("" ::: "memory");
#pragma unroll
    for (int pj = 0; pj < 4; ++pj) if (bad[pj]) v[pj] = ld16(rs, off + pj * pstride);
  }
  return pk_bf((g_val(v[0].x) + g_val(v[1].x)) + (g_val(v[2].x) + g_val(v[3].x)), (g_val(v[0].z) + g_val(v[1].z)) + (g_val(v[2].z) + g_val(v[3].z)));
}
DI Tag make_tag(u64 E) {
  const u64 T = E + 1;
  Tag tg;
  tg.hi = __builtin_amdgcn_readfirstlane((unsigned)(T >> 8));
  tg.lo8 = __builtin_amdgcn_readfirstlane((unsigned)T & 0xffu);
  tg.t32 = __builtin_amdgcn_readfirstlane((unsigned)T);
  tg.top = __builtin_amdgcn_readfirstlane((unsigned)(T >> 32));
  tg.nz = tg.t32 | 1u;
  return tg;
}

DI v4u poll_pair(rsrc_t rs, unsigned off, const Tag& tg) {
  v4u v = ld16(rs, off);
  unsigned spins = 0;
  while (!__all(g_ok2(v, tg)) && ++spins < SPIN_CAP) { __builtin_amdgcn_s_sleep(1); asm volatile("" ::: "memory"); v = ld16(rs, off); }
  return v;
}

DI void final_reduce(const Params& p, rsrc_t rs, const Tag& tg, int which, int a, int lane, u64 t0 = 0) {
  const int b = a >> 6, n0 = (a & 63) * 8, head = lane >> 1, hf = lane & 1;
  const unsigned off = WS_Y + ((((unsigned)which * 32 + head) * 2 + b) * 512 + n0 + 4 * hf) * 8;
  v4u v0 = ld16(rs, off), v1 = ld16(rs, off + 16);
  unsigned spins = 0;
  while (!(g_ok2(v0, tg) && g_ok2(v1, tg)) && ++spins < SPIN_CAP) { __builtin_amdgcn_s_sleep(1); asm volatile("" ::: "memory"); v0 = ld16(rs, off); v1 = ld16(rs, off + 16); }
  float r0 = swap32_add(g_val(v0.x), g_val(v1.x)), r1 = swap32_add(g_val(v0.z), g_val(v1.z));
  float x = swap16_add(r0, r1);
  x += dpp<0x4E>(x);
  x += dpp<0x124>(x);
  x += dpp<0x128>(x);
  if (t0 && a == 0 && lane == 0 && (__builtin_amdgcn_s_memrealtime() - t0) > (u64)XLIM) x += 1.0f;
  if ((lane & 15) < 2) p.out[b * 1024 + which * 512 + n0 + 4 * hf + (lane >> 4)] = (u16)f2bf_bits(x);
}

DI float score4(const v4u& k, const v4u& q) {
  float d = dot2(k.x, q.x, 0.f); d = dot2(k.y, q.y, d); d = dot2(k.z, q.z, d); d = dot2(k.w, q.w, d);
  return rbf(rbf(row16_sum(d)) * SCALE);
}
template <int N>
DI void pv_record(rsrc_t rs, const Tag& tg, const float (&s)[N], const v4u (&V)[N], float m, unsigned ST, unsigned mg, unsigned rec, int tid) {
  const int lane = tid & 63, w = __builtin_amdgcn_readfirstlane(tid >> 6), c16 = lane & 15, g = lane >> 4;
  m = xrow_max(m);
  float l = 0.f, acc[8];
#pragma unroll
  for (int k = 0; k < 8; ++k) acc[k] = 0.f;
  const float mneg = -m * LOG2E;
#pragma unroll
  for (int i = 0; i < N; ++i) {
    const float pp = __builtin_amdgcn_exp2f(fmaf(s[i], LOG2E, mneg));
    l += pp;
#pragma unroll
    for (int k = 0; k < 4; ++k) { acc[2 * k] = fmaf(pp, bflo(V[i][k]), acc[2 * k]); acc[2 * k + 1] = fmaf(pp, bfhi(V[i][k]), acc[2 * k + 1]); }
  }
  l = xrow_sum(l);
  float r[4], t[2];
#pragma unroll
  for (int k = 0; k < 4; ++k) r[k] = swap32_add(acc[k], acc[k + 4]);
#pragma unroll
  for (int k = 0; k < 2; ++k) t[k] = swap16_add(r[k], r[k + 2]);
  lds_st<v2u>(mg + w * 512 + (8 * c16 + 4 * (g >> 1) + 2 * (g & 1)) * 4, v2u{__float_as_uint(t[0]), __float_as_uint(t[1])});
  if (lane == 0) lds_st<v2u>(ST + w * 8, v2u{__float_as_uint(m), __float_as_uint(l)});
  wg_barrier();
  if (tid < 192) {
    const f32x4 s0 = lds_ld<f32x4>(ST), s1 = lds_ld<f32x4>(ST + 16), s2 = lds_ld<f32x4>(ST + 32), s3 = lds_ld<f32x4>(ST + 48);
    const float M = fmaxf(fmaxf(fmaxf(s0[0], s0[2]), fmaxf(s1[0], s1[2])), fmaxf(fmaxf(s2[0], s2[2]), fmaxf(s3[0], s3[2])));
    float e[8];
    e[0] = __builtin_amdgcn_exp2f((s0[0] - M) * LOG2E); e[1] = __builtin_amdgcn_exp2f((s0[2] - M) * LOG2E);
    e[2] = __builtin_amdgcn_exp2f((s1[0] - M) * LOG2E); e[3] = __builtin_amdgcn_exp2f((s1[2] - M) * LOG2E);
    e[4] = __builtin_amdgcn_exp2f((s2[0] - M) * LOG2E); e[5] = __builtin_amdgcn_exp2f((s2[2] - M) * LOG2E);
    e[6] = __builtin_amdgcn_exp2f((s3[0] - M) * LOG2E); e[7] = __builtin_amdgcn_exp2f((s3[2] - M) * LOG2E);
    if (tid < 128) {
      float o = 0.f;
#pragma unroll
      for (int ww = 0; ww < 8; ++ww) o = fmaf(e[ww], lds_ld<float>(mg + ww * 512 + tid * 4), o);
      st8(rs, rec + tid * 8, g_lo(o, tg), tg.hi);
    } else if (tid == 128) {
      const float L = e[0] * s0[1] + e[1] * s0[3] + e[2] * s1[1] + e[3] * s1[3] + e[4] * s2[1] + e[5] * s2[3] + e[6] * s3[1] + e[7] * s3[3];
      st16(rs, rec + 1024, v4u{g_lo(L, tg), tg.hi, g_lo(M, tg), tg.hi});
    }
  }
}

DI void stream_wg(const Params& p, rsrc_t rs, const u64 E, int h, int j, unsigned lb) {
  const int b = j >> 1, half = j & 1, bh = b * 32 + h;
  const int tid = threadIdx.x, lane = tid & 63, w = __builtin_amdgcn_readfirstlane(tid >> 6), c16 = lane & 15, g = lane >> 4;
  const size_t off = ((size_t)bh * 8192 + 6144 + half * 1024 + w * (4 * NY)) * 128;
  const rsrc_t rk = mk_rsrc(p.kc + off, NY * 1024), rv = mk_rsrc(p.vc + off, NY * 1024);
  dma_slice(p.wqB + (size_t)(128 * j) * 4096 + h * 128, 4096, lb + 32768, w, lane);
  bf16x8 xa[4];
#pragma unroll
  for (int s = 0; s < 4; ++s) xa[s] = *reinterpret_cast<const bf16x8*>(p.xB + (lane & 1) * 512 + 128 * j + 32 * s + 8 * g);
  v4u K[NY], V[NY];
#pragma unroll
  for (int i = 0; i < KINIT; ++i) K[i] = ldnt_b(rk, lane * 16, i * 1024);
  asm volatile("s_waitcnt vmcnt(%0)" :: "i"(KINIT) : "memory");
  wg_barrier();
  const Tag tg = make_tag(E);
  {
    const f32x4v d = gemv_mfma(xa, lb + 32768, w, lane);
    gemv_publish(rs, tg, d, WS_Q1 + (((unsigned)h * 4 + j) * 2 + 1) * 2048, 128, w, lane);
  }
  STAMP(1);
  v4u qa[4];
  const unsigned qoff = WS_Q1 + ((((unsigned)h * 4) * 2 + 1) * 2 + b) * 1024 + lane * 16;
#pragma unroll
  for (int i = KINIT; i < 2 * NY; ++i) {
    if (i < NY) K[i] = ldnt_b(rk, lane * 16, i * 1024); else V[i - NY] = ldnt_b(rv, lane * 16, (i - NY) * 1024);
    if (i == NY - 4) { if (w == 0) {
#pragma unroll
        for (int pj = 0; pj < 4; ++pj) qa[pj] = ld16(rs, qoff + pj * 4096); } }
    if (i == NY + 4) {
      if (w == 0) {
        unsigned spins = 0;
        while (!__all(g_ok2(qa[0], tg) && g_ok2(qa[1], tg) && g_ok2(qa[2], tg) && g_ok2(qa[3], tg)) && ++spins < SPIN_CAP) {
          asm volatile("" ::: "memory");
#pragma unroll
          for (int pj = 0; pj < 4; ++pj) qa[pj] = ld16(rs, qoff + pj * 4096);
        }
        const float q0 = (g_val(qa[0].x) + g_val(qa[1].x)) + (g_val(qa[2].x) + g_val(qa[3].x));
        const float q1 = (g_val(qa[0].z) + g_val(qa[1].z)) + (g_val(qa[2].z) + g_val(qa[3].z));
        lds_st<unsigned>(lb + lane * 4, pk_bf(q0, q1));
      }
    }
    if (i < KINIT + KSLOW) asm volatile("s_waitcnt vmcnt(%0)" :: "i"(SWIN) : "memory");
    else if (i >= KWIN) asm volatile("s_waitcnt vmcnt(%0)" :: "i"(KWIN) : "memory");
  }
  dma_slice(p.wuv + (size_t)(128 * j) * 4096 + h * 128, 4096, lb + 65536, w, lane);
  wg_barrier();
  STAMP(2);
  {
    const v4u q = lds_ld<v4u>(lb + c16 * 16);
    float s[NY], m = -INFINITY;
#pragma unroll
    for (int i = 0; i < NY; ++i) { s[i] = score4(K[i], q); m = fmaxf(m, s[i]); }
    dma_slice(p.woA + (size_t)(h * 128) * 512 + 128 * j, 512, lb + 98304, w, lane);
    STAMP(3);
    pv_record<NY>(rs, tg, s, V, m, lb + 512, lb + 1024, WS_REC + (((unsigned)h * 4 + j) * 2 + 0) * 1088, tid);
  }
  STAMP(4);
  const unsigned MLS = lb + 6144, V16 = lb + 7168, R2 = lb + 8192;
  if (tid < 128) {
    const v4u v = poll_pair(rs, WS_ML + ((unsigned)h * 128 + tid) * 16, tg);
    lds_st<v2u>(MLS + tid * 8, v2u{v.x << 8, v.z << 8});
  }
  wg_barrier();
  STAMP(5);
  {
    v4u pv[4];
#pragma unroll
    for (int i = 0; i < 4; ++i) pv[i] = ld16(rs, WS_PART + (((unsigned)(16 * w + 4 * i + g) * 32 + h) * 512 + 128 * j + 8 * c16) * 2);
    const int bb = w >> 2;
    const v2u ml = lds_ld<v2u>(MLS + (bb * 64 + lane) * 8);
    const float m = __uint_as_float(ml.x), l = __uint_as_float(ml.y);
    float M = m;
    M = fmaxf(M, dpp<0xB1>(M)); M = fmaxf(M, dpp<0x4E>(M)); M = fmaxf(M, dpp<0x141>(M)); M = fmaxf(M, dpp<0x140>(M)); M = xrow_max(M);
    const float e = __builtin_amdgcn_exp2f((m - M) * LOG2E);
    const float L = xrow_sum(row16_sum(e * l));
    const float wt = e / L;
    float wi[4];
#pragma unroll
    for (int i = 0; i < 4; ++i) wi[i] = __shfl(wt, (16 * w + 4 * i + g) & 63);
    float acc[8];
#pragma unroll
    for (int k = 0; k < 8; ++k) acc[k] = 0.f;
#pragma unroll
    for (int i = 0; i < 4; ++i) {
#pragma unroll
      for (int k = 0; k < 4; ++k) { acc[2 * k] = fmaf(wi[i], bflo(pv[i][k]), acc[2 * k]); acc[2 * k + 1] = fmaf(wi[i], bfhi(pv[i][k]), acc[2 * k + 1]); }
    }
    float r[4], t[2];
#pragma unroll
    for (int k = 0; k < 4; ++k) r[k] = swap32_add(acc[k], acc[k + 4]);
#pragma unroll
    for (int k = 0; k < 2; ++k) t[k] = swap16_add(r[k], r[k + 2]);
    lds_st<v2u>(R2 + (w * 128 + 8 * c16 + 4 * (g >> 1) + 2 * (g & 1)) * 4, v2u{__float_as_uint(t[0]), __float_as_uint(t[1])});
  }
  wg_barrier();
  if (tid < 128) {
    const int bb = tid >> 6, cp = tid & 63;
    float x0 = 0.f, x1 = 0.f;
#pragma unroll
    for (int ww = 0; ww < 4; ++ww) { const v2u t = lds_ld<v2u>(R2 + ((4 * bb + ww) * 128 + 2 * cp) * 4); x0 += __uint_as_float(t.x); x1 += __uint_as_float(t.y); }
    lds_st<unsigned>(V16 + tid * 4, pk_bf(x0, x1));
  }
  wg_barrier();
  {
    bf16x8 f[4];
    vec_frags(f, V16, lane);
    const f32x4v d = gemv_mfma(f, lb + 65536, w, lane);
    gemv_publish(rs, tg, d, WS_C4 + ((unsigned)h * 4 + j) * 2048, 128, w, lane);
  }
  STAMP(6);
  if (tid < 128) {
    const unsigned v = gather4(rs, tg, WS_C4 + ((unsigned)h * 4) * 2048 + (tid >> 6) * 1024 + (tid & 63) * 16, 2048);
    lds_st<unsigned>(V16 + 512 + tid * 4, v);
  }
  wg_barrier();
  {
    bf16x8 f[4];
    vec_frags(f, V16 + 512, lane);
    const f32x4v d = gemv_mfma(f, lb + 98304, w, lane);
    gemv_publish(rs, tg, d, WS_Y + (((unsigned)0 * 32 + h) * 1024 + 128 * j) * 8, 512, w, lane);
  }
  STAMP(7);
  if (w == 0) final_reduce(p, rs, tg, 0, h * 4 + j, lane);
  STAMP(8);
}

DI void chain_wg(const Params& p, rsrc_t rs, const u64 E, int a, unsigned lb) {
  const u64 tstart = __builtin_amdgcn_s_memrealtime();
  const int h = a >> 2, j = a & 3;
  const int tid = threadIdx.x, lane = tid & 63, w = __builtin_amdgcn_readfirstlane(tid >> 6), g = lane >> 4, c16 = lane & 15;
  const unsigned scr = lb + SCR;
  dma_slice(p.wqA + (size_t)(128 * j) * 4096 + h * 128, 4096, scr, w, lane);
  bf16x8 fa[4], WK[4];
#pragma unroll
  for (int s = 0; s < 4; ++s) fa[s] = *reinterpret_cast<const bf16x8*>(p.xA + (lane & 1) * 512 + 128 * j + 32 * s + 8 * g);
  {
    const rsrc_t rw = mk_rsrc(p.wuk + (size_t)(128 * j + 16 * w) * 4096 + h * 128, 16 * 8192);
#pragma unroll
    for (int s = 0; s < 4; ++s) WK[s] = __builtin_bit_cast(bf16x8, ldnt_b(rw, c16 * 8192 + g * 16, 64 * s));
  }
  const int bA = a >> 6;
  asm volatile("s_barrier" ::: "memory");
  dma_rows1k<0, CK0>(p.ckv + (size_t)a * 128 * 512, lb, w, lane);
  asm volatile("s_waitcnt vmcnt(%0)" :: "i"(4 + CK0) : "memory");
  wg_barrier();
  const Tag tg = make_tag(E);
  {
    const f32x4v d = gemv_mfma(fa, scr, w, lane);
    gemv_publish(rs, tg, d, WS_Q1 + (((unsigned)h * 4 + j) * 2 + 0) * 2048, 128, w, lane);
  }
  STAMP(1);
  dma_rows1k<CK0, CK1>(p.ckv + (size_t)a * 128 * 512, lb, w, lane);
  wg_barrier();
  if (tid < 128) {
    const unsigned v = gather4(rs, tg, WS_Q1 + (((unsigned)h * 4) * 4 + (tid >> 6)) * 1024 + (tid & 63) * 16, 4096);
    lds_st<unsigned>(scr + tid * 4, v);
  } else if (tid < 192) {
    const unsigned v = gather4(rs, tg, WS_Q1 + (((unsigned)h * 4) * 4 + 2 + (j >> 1)) * 1024 + lane * 16, 4096);
    lds_st<unsigned>(scr + 512 + lane * 4, v);
  }
  wg_barrier();
  STAMP(2);
  const v4u qB = lds_ld<v4u>(scr + 512 + c16 * 16);
  {
    bf16x8 qf[4];
    vec_frags(qf, scr, lane);
    f32x4v d = {0.f, 0.f, 0.f, 0.f};
#pragma unroll
    for (int s = 0; s < 4; ++s) d = __builtin_amdgcn_mfma_f32_16x16x32_bf16(WK[s], qf[s], d, 0, 0, 0);
    if (c16 < 2)
      st16(rs, WS_QL + (((unsigned)c16 * 32 + h) * 256 + 64 * j + 8 * w + 2 * g) * 8, v4u{pk_bf(d[0], d[1]), tg.t32, pk_bf(d[2], d[3]), tg.t32});
  }
  STAMP(3);
  dma_rows1k<CK1, 16>(p.ckv + (size_t)a * 128 * 512, lb, w, lane);
  const int r32 = lane & 31, hh = lane >> 5;
  float sc[8];
  s16x4 xt[32];
  bf16x8 ka[16];
  const unsigned fr = swz(r32);
  {
    v4u v[8];
    const unsigned base = WS_QL + (unsigned)bA * 65536 + tid * 16;
#pragma unroll
    for (int i = 0; i < 8; ++i) { v[i] = ld16(rs, base + i * 8192); }
    asm volatile("s_waitcnt vmcnt(8)" ::: "memory");
    wg_barrier();
    STAMP(11);
    {
      const unsigned abase = lb + (32 * (w & 3) + r32) * 1024 + 512 * (w >> 2);
#pragma unroll
      for (int s = 0; s < 16; ++s) ka[s] = lds_ld<bf16x8>(abase + 16 * ((unsigned)((2 * s + hh) & 15) ^ fr) + 256 * (s >> 3));
      const unsigned blk = (lane >> 4) & 1, q = (lane & 15) >> 2, pp = lane & 3;
      unsigned tb[4];
#pragma unroll
      for (int mt = 0; mt < 2; ++mt)
#pragma unroll
        for (int t = 0; t < 2; ++t) {
          const unsigned row = 8 * hh + 4 * t + q, chn = 8 * w + 4 * mt + 2 * blk + (pp >> 1);
          tb[mt * 2 + t] = lb + row * 1024 + 16 * (chn ^ swz(row)) + 8 * (pp & 1);
        }
#pragma unroll
      for (int hf = 0; hf < 2; ++hf) {
        asm volatile(
            "ds_read_b64_tr_b16 %0, %16\n\tds_read_b64_tr_b16 %1, %17\n\tds_read_b64_tr_b16 %2, %18\n\tds_read_b64_tr_b16 %3, %19\n\t"
            "ds_read_b64_tr_b16 %4, %16 offset:16384\n\tds_read_b64_tr_b16 %5, %17 offset:16384\n\tds_read_b64_tr_b16 %6, %18 offset:16384\n\tds_read_b64_tr_b16 %7, %19 offset:16384\n\t"
            "ds_read_b64_tr_b16 %8, %16 offset:32768\n\tds_read_b64_tr_b16 %9, %17 offset:32768\n\tds_read_b64_tr_b16 %10, %18 offset:32768\n\tds_read_b64_tr_b16 %11, %19 offset:32768\n\t"
            "ds_read_b64_tr_b16 %12, %16 offset:49152\n\tds_read_b64_tr_b16 %13, %17 offset:49152\n\tds_read_b64_tr_b16 %14, %18 offset:49152\n\tds_read_b64_tr_b16 %15, %19 offset:49152\n\t"
            "s_waitcnt lgkmcnt(0)"
            : "=&v"(xt[16 * hf + 0]), "=&v"(xt[16 * hf + 1]), "=&v"(xt[16 * hf + 2]), "=&v"(xt[16 * hf + 3]),
              "=&v"(xt[16 * hf + 4]), "=&v"(xt[16 * hf + 5]), "=&v"(xt[16 * hf + 6]), "=&v"(xt[16 * hf + 7]),
              "=&v"(xt[16 * hf + 8]), "=&v"(xt[16 * hf + 9]), "=&v"(xt[16 * hf + 10]), "=&v"(xt[16 * hf + 11]),
              "=&v"(xt[16 * hf + 12]), "=&v"(xt[16 * hf + 13]), "=&v"(xt[16 * hf + 14]), "=&v"(xt[16 * hf + 15])
            : "v"(tb[0] + hf * 65536), "v"(tb[1] + hf * 65536), "v"(tb[2] + hf * 65536), "v"(tb[3] + hf * 65536) : "memory");
      }
    }
    STAMP(12);
    unsigned spins = 0;
    const unsigned cq = tid & 127, h0 = tid >> 7;
    for (;;) {
      bool bad[8], any = false;
#pragma unroll
      for (int i = 0; i < 8; ++i) { bad[i] = !__all(v[i].y == tg.t32 && v[i].w == tg.t32); any = any || bad[i]; }
      if (!any || ++spins >= SPIN_CAP) break;
      asm volatile("" ::: "memory");
#pragma unroll
      for (int i = 0; i < 8; ++i) if (bad[i]) v[i] = ld16(rs, base + i * 8192);
    }
#pragma unroll
    for (int i = 0; i < 8; ++i) {
      const unsigned head = 4 * i + h0;
      lds_st<v2u>(scr + head * 1024 + 16 * ((cq >> 1) ^ swz(head)) + 8 * (cq & 1), v2u{v[i].x, v[i].z});
    }
  }
  wg_barrier();
  STAMP(4);
  const size_t koff = ((size_t)((j >> 1) * 32 + h) * 8192 + 6144 + (j & 1) * 1024 + 32 * NY + w * (4 * NX)) * 128;
  const rsrc_t rk = mk_rsrc(p.kc + koff, NX * 1024), rv = mk_rsrc(p.vc + koff, NX * 1024);
  v4u K[NX], V[NX];
#pragma unroll
  for (int i = 0; i < 7; ++i) K[i] = ldnt_b(rk, lane * 16, i * 1024);
  {
    const int ch = w >> 2;
    f32x16 acc;
#pragma unroll
    for (int i = 0; i < 16; ++i) acc[i] = 0.f;
    const unsigned bbase = scr + r32 * 1024 + 512 * ch;
#pragma unroll
    for (int s = 0; s < 16; ++s) {
      const bf16x8 bv = lds_ld<bf16x8>(bbase + 16 * ((unsigned)((2 * s + hh) & 15) ^ fr) + 256 * (s >> 3));
      acc = __builtin_amdgcn_mfma_f32_32x32x16_bf16(ka[s], bv, acc, 0, 0, 0);
      if ((s & 7) == 7) __builtin_amdgcn_sched_barrier(0);
    }
    f32x4 s0, s1;
#pragma unroll
    for (int k = 0; k < 4; ++k) { s0[k] = ch ? acc[k] : acc[8 + k]; s1[k] = ch ? acc[4 + k] : acc[12 + k]; }
    const unsigned XB = lb + 32768;
    lds_st<f32x4>(XB + w * 2048 + lane * 16, s0);
    lds_st<f32x4>(XB + w * 2048 + 1024 + lane * 16, s1);
    wg_barrier();
    const f32x4 t0 = lds_ld<f32x4>(XB + (w ^ 4) * 2048 + lane * 16), t1 = lds_ld<f32x4>(XB + (w ^ 4) * 2048 + 1024 + lane * 16);
#pragma unroll
    for (int k = 0; k < 4; ++k) { sc[k] = ((ch ? acc[8 + k] : acc[k]) + t0[k]) * SCALE; sc[4 + k] = ((ch ? acc[12 + k] : acc[4 + k]) + t1[k]) * SCALE; }
  }
#pragma unroll
  for (int i = 7; i < NX; ++i) K[i] = ldnt_b(rk, lane * 16, i * 1024);
  const unsigned MX = scr + 16384, LS = scr + 17408, PB = scr + 18432;
  float Mh;
  {
    float m = sc[0];
#pragma unroll
    for (int k = 1; k < 8; ++k) m = fmaxf(m, sc[k]);
    { auto t = __builtin_amdgcn_permlane32_swap(__float_as_uint(m), __float_as_uint(m), false, false); m = fmaxf(__uint_as_float(t[0]), __uint_as_float(t[1])); }
    if (lane < 32) lds_st<float>(MX + (lane * 8 + w) * 4, m);
    wg_barrier();
    const f32x4 ma = lds_ld<f32x4>(MX + r32 * 32), mb = lds_ld<f32x4>(MX + r32 * 32 + 16);
    float M = fmaxf(fmaxf(fmaxf(ma[0], ma[1]), fmaxf(ma[2], ma[3])), fmaxf(fmaxf(mb[0], mb[1]), fmaxf(mb[2], mb[3])));
    M = round24(M);
    Mh = M;
    const float mneg = -M * LOG2E;
    unsigned pk[4]; float l = 0.f;
#pragma unroll
    for (int k = 0; k < 4; ++k) {
      const float p0 = __builtin_amdgcn_exp2f(fmaf(sc[2 * k], LOG2E, mneg)), p1 = __builtin_amdgcn_exp2f(fmaf(sc[2 * k + 1], LOG2E, mneg));
      pk[k] = pk_bf(p0, p1);
      l += bflo(pk[k]) + bfhi(pk[k]);
    }
    { auto t = __builtin_amdgcn_permlane32_swap(__float_as_uint(l), __float_as_uint(l), false, false); l = __uint_as_float(t[0]) + __uint_as_float(t[1]); }
    if (lane < 32) lds_st<float>(LS + (lane * 8 + w) * 4, l);
    const int kt = w & 3, ch = w >> 2;
    lds_st<v2u>(PB + r32 * 272 + (32 * kt + 16 * ch + 4 * hh) * 2, v2u{pk[0], pk[1]});
    lds_st<v2u>(PB + r32 * 272 + (32 * kt + 16 * ch + 8 + 4 * hh) * 2, v2u{pk[2], pk[3]});
  }
  wg_barrier();
  f32x16 o0, o1;
#pragma unroll
  for (int i = 0; i < 16; ++i) { o0[i] = 0.f; o1[i] = 0.f; }
#pragma unroll
  for (int s = 0; s < 8; ++s) {
    const bf16x8 pv = lds_ld<bf16x8>(PB + r32 * 272 + 32 * s + 16 * hh);
    o0 = __builtin_amdgcn_mfma_f32_32x32x16_bf16(__builtin_shufflevector(xt[4 * s], xt[4 * s + 1], 0, 1, 2, 3, 4, 5, 6, 7), pv, o0, 0, 0, 0);
    o1 = __builtin_amdgcn_mfma_f32_32x32x16_bf16(__builtin_shufflevector(xt[4 * s + 2], xt[4 * s + 3], 0, 1, 2, 3, 4, 5, 6, 7), pv, o1, 0, 0, 0);
  }
  STAMP(5);
  {
    const unsigned tb = lb + 32768 + w * 4096;
#pragma unroll
    for (int gq = 0; gq < 4; ++gq) {
      lds_st<v2u>(tb + r32 * 128 + 16 * (gq ^ (r32 & 7)) + 8 * hh, v2u{pk_bf(o0[4 * gq], o0[4 * gq + 1]), pk_bf(o0[4 * gq + 2], o0[4 * gq + 3])});
      lds_st<v2u>(tb + r32 * 128 + 16 * ((4 + gq) ^ (r32 & 7)) + 8 * hh, v2u{pk_bf(o1[4 * gq], o1[4 * gq + 1]), pk_bf(o1[4 * gq + 2], o1[4 * gq + 3])});
    }
    asm volatile("s_waitcnt lgkmcnt(0)" ::: "memory");
#pragma unroll
    for (int i = 0; i < 4; ++i) {
      const int head = 8 * i + (lane >> 3);
      const v4u v = lds_ld<v4u>(tb + head * 128 + 16 * ((lane & 7) ^ (head & 7)));
      st16(rs, WS_PART + (((unsigned)a * 32 + head) * 512 + 64 * w + 8 * (lane & 7)) * 2, v);
    }
    asm volatile("" ::: "memory");
#pragma unroll
    for (int i = 0; i < 6; ++i) V[i] = ldnt_b(rv, lane * 16, i * 1024);
    asm volatile("s_waitcnt vmcnt(6)" ::: "memory");
  }
  wg_barrier();
  if (w == 0 && lane < 32) {
    const f32x4 la = lds_ld<f32x4>(LS + lane * 32), lc = lds_ld<f32x4>(LS + lane * 32 + 16);
    const float l = ((la[0] + la[1]) + (la[2] + la[3])) + ((lc[0] + lc[1]) + (lc[2] + lc[3]));
    st16(rs, WS_ML + ((unsigned)lane * 128 + a) * 16, v4u{g_lo(Mh, tg), tg.hi, g_lo(l, tg), tg.hi});
  }
  STAMP(6);
  if (a == 0 && w == 7) {
    v4u d0 = ld16(rs, WS_Q1 + ((unsigned)lane * 2 + 1) * 2048), d1 = ld16(rs, WS_Q1 + ((unsigned)(lane + 64) * 2 + 1) * 2048);
    unsigned spins = 0;
    while (!__all(g_ok2(d0, tg) && g_ok2(d1, tg)) && ++spins < SPIN_CAP) {
      __builtin_amdgcn_s_sleep(2); asm volatile("" ::: "memory");
      d0 = ld16(rs, WS_Q1 + ((unsigned)lane * 2 + 1) * 2048); d1 = ld16(rs, WS_Q1 + ((unsigned)(lane + 64) * 2 + 1) * 2048);
    }
    if (lane == 0) __hip_atomic_store(reinterpret_cast<u64*>(p.ws + WS_EPOCH), ((u64)tg.top << 32) | tg.t32, __ATOMIC_RELAXED, __HIP_MEMORY_SCOPE_AGENT);
  }
#pragma unroll
  for (int i = 6; i < NX; ++i) V[i] = ldnt_b(rv, lane * 16, i * 1024);
  {
    float s[NX], m = -INFINITY;
#pragma unroll
    for (int i = 0; i < NX; ++i) { s[i] = score4(K[i], qB); m = fmaxf(m, s[i]); }
    dma_slice(p.woB + (size_t)(h * 128) * 512 + 128 * j, 512, lb, w, lane);
    STAMP(7);
    pv_record<NX>(rs, tg, s, V, m, lb + 65536, lb + 66560, WS_REC + (((unsigned)h * 4 + j) * 2 + 1) * 1088, tid);
  }
  STAMP(8);
  const unsigned V16 = lb + 71680;
  if (tid < 128) {
    const int bb = tid >> 6, dp = tid & 63;
    const unsigned r0 = WS_REC + (((unsigned)h * 4 + 2 * bb) * 2) * 1088;
    v4u o[4], st[4];
#pragma unroll
    for (int r = 0; r < 4; ++r) { o[r] = ld16(rs, r0 + r * 1088 + dp * 16); st[r] = ld16(rs, r0 + r * 1088 + 1024); }
    unsigned spins = 0;
    for (;;) {
      bool bad[4], any = false;
#pragma unroll
      for (int r = 0; r < 4; ++r) { bad[r] = !__all(g_ok2(o[r], tg) && g_ok2(st[r], tg)); any = any || bad[r]; }
      if (!any || ++spins >= SPIN_CAP) break;
      asm volatile("" ::: "memory");
#pragma unroll
      for (int r = 0; r < 4; ++r) if (bad[r]) { o[r] = ld16(rs, r0 + r * 1088 + dp * 16); st[r] = ld16(rs, r0 + r * 1088 + 1024); }
    }
    const float Mx = fmaxf(fmaxf(g_val(st[0].z), g_val(st[1].z)), fmaxf(g_val(st[2].z), g_val(st[3].z)));
    float L = 0.f, o0 = 0.f, o1 = 0.f;
#pragma unroll
    for (int r = 0; r < 4; ++r) {
      const float e = __builtin_amdgcn_exp2f((g_val(st[r].z) - Mx) * LOG2E);
      L = fmaf(e, g_val(st[r].x), L); o0 = fmaf(e, g_val(o[r].x), o0); o1 = fmaf(e, g_val(o[r].z), o1);
    }
    const float inv = 1.f / L;
    lds_st<unsigned>(V16 + tid * 4, pk_bf(o0 * inv, o1 * inv));
  }
  asm volatile("s_waitcnt vmcnt(0)" ::: "memory");
  wg_barrier();
  STAMP(9);
  {
    bf16x8 f[4];
    vec_frags(f, V16, lane);
    const f32x4v d = gemv_mfma(f, lb, w, lane);
    gemv_publish(rs, tg, d, WS_Y + (((unsigned)1 * 32 + h) * 1024 + 128 * j) * 8, 512, w, lane);
  }
  if (w == 0) final_reduce(p, rs, tg, 1, a, lane, tstart);
  STAMP(10);
}

__global__ void __launch_bounds__(512, 2) fused_decode_kernel(Params p) {
  extern __shared__ __attribute__((aligned(16))) char smem[];
  const unsigned lb = (unsigned)(uintptr_t)(LAS char*)smem;
  STAMP(0);
  u64 pc; asm volatile("s_getpc_b64 %0" : "=s"(pc));
  const rsrc_t rs = __builtin_amdgcn_make_buffer_rsrc(p.ws, 0, WS_END, 0x00020000);
  const u64 E = __hip_atomic_load(reinterpret_cast<u64*>(p.ws + WS_EPOCH), __ATOMIC_RELAXED, __HIP_MEMORY_SCOPE_AGENT);
  const int gidx = blockIdx.x;
  const int role = (gidx >> 3) & 1, k = gidx >> 4, head = 4 * (gidx & 7) + (k >> 2), j = k & 3;
  if (role == 0) {
    const unsigned o = (threadIdx.x * 64u < CODE_BYTES) ? threadIdx.x * 64u : 0u;
    const u64 ad = (pc & ~63ull) + o;
    unsigned keep;
    asm volatile("s_mov_b32 %0, m0\n\ts_mov_b32 m0, %2\n\ts_nop 0\n\tglobal_load_lds_dword %1, off\n\ts_mov_b32 m0, %0"
                 : "=&s"(keep) : "v"(ad), "s"(lb + 16384u + (unsigned)__builtin_amdgcn_readfirstlane(threadIdx.x >> 6) * 256u) : "memory");
    stream_wg(p, rs, E, head, j, lb);
  }
  else chain_wg(p, rs, E, head * 4 + j, lb);
}

extern "C" void kernel_launch(void* const* d_in, const int* in_sizes, int n_in, void* d_out, int out_size, void* d_ws, size_t ws_size,
                              hipStream_t stream) {
  Params p;
  p.xA = (const u16*)d_in[0]; p.ckv = (const u16*)d_in[1]; p.wqA = (const u16*)d_in[2]; p.wuk = (const u16*)d_in[3];
  p.wuv = (const u16*)d_in[4]; p.woA = (const u16*)d_in[5]; p.xB = (const u16*)d_in[6]; p.kc = (const u16*)d_in[7];
  p.vc = (const u16*)d_in[8]; p.wqB = (const u16*)d_in[9]; p.woB = (const u16*)d_in[10];
  p.out = (u16*)d_out; p.ws = (unsigned char*)d_ws;
  (void)hipFuncSetAttribute((const void*)fused_decode_kernel, hipFuncAttributeMaxDynamicSharedMemorySize, LDS_BYTES);
  fused_decode_kernel<<<256, 512, LDS_BYTES, stream>>>(p);
}
```

```cpp
#include <hip/hip_runtime.h>
#include <stdint.h>
#define SHPOL " nt"

typedef unsigned short u16;
typedef unsigned long long u64;
typedef unsigned v4u __attribute__((ext_vector_type(4)));
typedef unsigned v2u __attribute__((ext_vector_type(2)));
typedef float f32x4 __attribute__((ext_vector_type(4)));
typedef float f32x16 __attribute__((ext_vector_type(16)));
typedef short bf16x8 __attribute__((ext_vector_type(8)));
typedef short s16x4 __attribute__((ext_vector_type(4)));
typedef __bf16 bf16x2 __attribute__((ext_vector_type(2)));
#define LAS __attribute__((address_space(3)))
#define DI __device__ __forceinline__

constexpr unsigned WS_EPOCH = 0;
constexpr unsigned WS_Q1 = 4096;
constexpr unsigned WS_QL = WS_Q1 + 65536u * 8;
constexpr unsigned WS_ML = WS_QL + 16384u * 8;
constexpr unsigned WS_C4 = WS_ML + 8192u * 8;
constexpr unsigned WS_REC = WS_C4 + 32768u * 8;
constexpr unsigned WS_Y = WS_REC + 32u * 8 * 136 * 8;
constexpr unsigned WS_PART = WS_Y + 65536u * 8;
constexpr unsigned WS_STAMP = WS_PART + 128u * 32 * 512 * 2;
constexpr unsigned WS_END = WS_STAMP + 256u * 16 * 8;

constexpr unsigned CODE_BYTES = 16384;
constexpr int LDS_BYTES = 163840;
constexpr unsigned SCR = 131072;
constexpr unsigned SPIN_CAP = 1u << 16;
#define KINIT_ 1
#define KWIN_ 8
#define CK1_ 12
#define VPRE_ 8
#define KSLOW_ 3
#define SWIN_ 0
#define CK0_ 6
constexpr int CK0 = CK0_;
#define NY_ 20
constexpr int NY = NY_, NX = 32 - NY_;
constexpr int KSLOW = KSLOW_, SWIN = SWIN_;
constexpr int KINIT = KINIT_, KWIN = KWIN_, CK1 = CK1_, VPRE = VPRE_;
constexpr float SCALE = 0.08838834764831845f;
constexpr float LOG2E = 1.4426950408889634f;

struct Params {
  const u16 *xA, *ckv, *wqA, *wuk, *wuv, *woA, *xB, *kc, *vc, *wqB, *woB;
  u16* out;
  unsigned char* ws;
};

DI float bflo(unsigned w) { return __uint_as_float(w << 16); }
DI float bfhi(unsigned w) { return __uint_as_float(w & 0xffff0000u); }
DI unsigned f2bf_bits(float f) { return (unsigned)__builtin_bit_cast(unsigned short, (__bf16)f); }
DI float rbf(float f) { return __uint_as_float(f2bf_bits(f) << 16); }
DI unsigned pk_bf(float lo, float hi) { return f2bf_bits(lo) | (f2bf_bits(hi) << 16); }
DI float dot2(unsigned a, unsigned b, float acc) {
  return __builtin_amdgcn_fdot2_f32_bf16(__builtin_bit_cast(bf16x2, a), __builtin_bit_cast(bf16x2, b), acc, false);
}
template <int CTRL> DI float dpp(float x) {
  return __builtin_bit_cast(float, __builtin_amdgcn_mov_dpp(__builtin_bit_cast(int, x), CTRL, 0xf, 0xf, true));
}
DI float row16_sum(float x) { x += dpp<0xB1>(x); x += dpp<0x4E>(x); x += dpp<0x141>(x); x += dpp<0x140>(x); return x; }
DI float xrow_max(float x) {
  auto s = __builtin_amdgcn_permlane16_swap(__float_as_uint(x), __float_as_uint(x), false, false);
  x = fmaxf(__uint_as_float(s[0]), __uint_as_float(s[1]));
  auto t = __builtin_amdgcn_permlane32_swap(__float_as_uint(x), __float_as_uint(x), false, false);
  return fmaxf(__uint_as_float(t[0]), __uint_as_float(t[1]));
}
DI float xrow_sum(float x) {
  auto s = __builtin_amdgcn_permlane16_swap(__float_as_uint(x), __float_as_uint(x), false, false);
  x = __uint_as_float(s[0]) + __uint_as_float(s[1]);
  auto t = __builtin_amdgcn_permlane32_swap(__float_as_uint(x), __float_as_uint(x), false, false);
  return __uint_as_float(t[0]) + __uint_as_float(t[1]);
}
DI float swap32_add(float a, float b) {
  auto t = __builtin_amdgcn_permlane32_swap(__float_as_uint(a), __float_as_uint(b), false, false);
  return __uint_as_float(t[0]) + __uint_as_float(t[1]);
}
DI float swap16_add(float a, float b) {
  auto t = __builtin_amdgcn_permlane16_swap(__float_as_uint(a), __float_as_uint(b), false, false);
  return __uint_as_float(t[0]) + __uint_as_float(t[1]);
}
DI v4u ldnt(const u16* p) { return __builtin_nontemporal_load(reinterpret_cast<const v4u*>(p)); }
DI void wg_barrier() { asm volatile("s_waitcnt lgkmcnt(0)\n\ts_barrier" ::: "memory"); }
DI void glds16_nt(const void* gsrc, unsigned lds_dst) {
  unsigned keep;
  asm volatile("s_mov_b32 %0, m0\n\ts_mov_b32 m0, %2\n\ts_nop 0\n\tglobal_load_lds_dwordx4 %1, off nt\n\ts_mov_b32 m0, %0"
               : "=&s"(keep) : "v"(gsrc), "s"(lds_dst) : "memory");
}
DI void glds16_def(const void* gsrc, unsigned lds_dst) {
  unsigned keep;
  asm volatile("s_mov_b32 %0, m0\n\ts_mov_b32 m0, %2\n\ts_nop 0\n\tglobal_load_lds_dwordx4 %1, off" SHPOL "\n\ts_mov_b32 m0, %0"
               : "=&s"(keep) : "v"(gsrc), "s"(lds_dst) : "memory");
}
typedef __amdgpu_buffer_rsrc_t rsrc_t;
DI rsrc_t mk_rsrc(const void* base, unsigned bytes) {
  const u64 b = (u64)base;
  const unsigned lo = __builtin_amdgcn_readfirstlane((unsigned)b), hi = __builtin_amdgcn_readfirstlane((unsigned)(b >> 32));
  return __builtin_amdgcn_make_buffer_rsrc((void*)(((u64)hi << 32) | lo), 0, bytes, 0x00020000);
}
DI v4u ldnt_b(rsrc_t r, unsigned voff, unsigned soff) { return __builtin_amdgcn_raw_buffer_load_b128(r, voff, soff, 2); }
DI v4u ld16(rsrc_t r, unsigned off) { return __builtin_amdgcn_raw_buffer_load_b128(r, off, 0, 16); }
DI void st8(rsrc_t r, unsigned off, unsigned lo, unsigned hi) { v2u v = {lo, hi}; __builtin_amdgcn_raw_buffer_store_b64(v, r, off, 0, 16); }
DI void st16(rsrc_t r, unsigned off, v4u v) { __builtin_amdgcn_raw_buffer_store_b128(v, r, off, 0, 16); }

struct Tag { unsigned hi, lo8, t32, top, nz; };
DI unsigned g_lo(float v, const Tag& t) { return ((__float_as_uint(v) + 0x80u) >> 8) | (t.lo8 << 24); }
DI float g_val(unsigned lo) { return __uint_as_float(lo << 8); }
DI bool g_ok(unsigned lo, unsigned hi, const Tag& t) { return hi == t.hi && (lo >> 24) == t.lo8; }
DI bool g_ok2(const v4u& v, const Tag& t) { return g_ok(v.x, v.y, t) && g_ok(v.z, v.w, t); }
DI float round24(float v) { return __uint_as_float((__float_as_uint(v) + 0x80u) & 0xffffff00u); }
DI unsigned swz(unsigned r) { return ((r & 3u) << 2) | ((r >> 2) & 3u); }

#define STAMP(k) do { } while (0)

template <typename T> DI void lds_st(unsigned addr, T v) { *(LAS T*)(uintptr_t)addr = v; }
template <typename T> DI T lds_ld(unsigned addr) { return *(LAS T*)(uintptr_t)addr; }

typedef float f32x4v __attribute__((ext_vector_type(4)));

DI void dma_slice(const u16* src0, size_t row_stride, unsigned dst, int w, int lane) {
  asm volatile("" : "+v"(lane));
  const int g = lane >> 4, c16 = lane & 15;
  const u16* s = src0 + (size_t)(16 * w + g) * row_stride;
#pragma unroll
  for (int i = 0; i < 4; ++i) glds16_nt(s + (size_t)(4 * i) * row_stride + 8 * (c16 ^ ((g << 2) | i)), dst + (16 * w + 4 * i) * 256);
}
DI f32x4v gemv_mfma(const bf16x8 (&a)[4], unsigned wbase, int w, int lane) {
  const unsigned g = lane >> 4, q = (lane & 15) >> 2, pp = lane & 3;
  const unsigned chn = 2 * w + (pp >> 1);
  const unsigned a0 = wbase + (8 * g + q) * 256 + 16 * (chn ^ ((q << 2) | ((2 * g) & 3))) + 8 * (pp & 1);
  const unsigned a1 = wbase + (8 * g + 4 + q) * 256 + 16 * (chn ^ ((q << 2) | ((2 * g + 1) & 3))) + 8 * (pp & 1);
  s16x4 x[8];
  asm volatile("ds_read_b64_tr_b16 %0, %8\n\tds_read_b64_tr_b16 %1, %9\n\t"
               "ds_read_b64_tr_b16 %2, %8 offset:8192\n\tds_read_b64_tr_b16 %3, %9 offset:8192\n\t"
               "ds_read_b64_tr_b16 %4, %8 offset:16384\n\tds_read_b64_tr_b16 %5, %9 offset:16384\n\t"
               "ds_read_b64_tr_b16 %6, %8 offset:24576\n\tds_read_b64_tr_b16 %7, %9 offset:24576\n\ts_waitcnt lgkmcnt(0)"
               : "=&v"(x[0]), "=&v"(x[1]), "=&v"(x[2]), "=&v"(x[3]), "=&v"(x[4]), "=&v"(x[5]), "=&v"(x[6]), "=&v"(x[7])
               : "v"(a0), "v"(a1) : "memory");
  f32x4v d = {0.f, 0.f, 0.f, 0.f};
#pragma unroll
  for (int s = 0; s < 4; ++s)
    d = __builtin_amdgcn_mfma_f32_16x16x32_bf16(a[s], __builtin_shufflevector(x[2 * s], x[2 * s + 1], 0, 1, 2, 3, 4, 5, 6, 7), d, 0, 0, 0);
  return d;
}

template <int I0, int I1, bool NT = true> DI void dma_rows1k(const u16* src, unsigned dst, int w, int lane) {
  asm volatile("" : "+v"(lane));
  const u16* s = src + (size_t)(16 * w) * 512;
#pragma unroll
  for (int i = I0; i < I1; ++i) {
    if (NT) glds16_nt(s + i * 512 + 8 * (lane ^ swz(i)), dst + (16 * w + i) * 1024);
    else glds16_def(s + i * 512 + 8 * (lane ^ swz(i)), dst + (16 * w + i) * 1024);
  }
}
DI void vec_frags(bf16x8 (&a)[4], unsigned vbase, int lane) {
  const unsigned ad = vbase + (lane & 1) * 256 + (lane >> 4) * 16;
#pragma unroll
  for (int s = 0; s < 4; ++s) a[s] = lds_ld<bf16x8>(ad + 64 * s);
}
DI void gemv_publish(rsrc_t rs, const Tag& tg, const f32x4v& d, unsigned base, unsigned bstride, int w, int lane) {
  if (lane < 32) {
    const float v = lane < 16 ? d[0] : d[1];
    st8(rs, base + ((lane >> 4) * bstride + 16 * w + (lane & 15)) * 8, g_lo(v, tg), tg.hi);
  }
}
DI unsigned gather4(rsrc_t rs, const Tag& tg, unsigned off, unsigned pstride) {
  v4u v[4];
#pragma unroll
  for (int pj = 0; pj < 4; ++pj) v[pj] = ld16(rs, off + pj * pstride);
  unsigned spins = 0;
  for (;;) {
    bool bad[4], any = false;
#pragma unroll
    for (int pj = 0; pj < 4; ++pj) { bad[pj] = !__all(g_ok2(v[pj], tg)); any = any || bad[pj]; }
    if (!any || ++spins >= SPIN_CAP) break;
    asm volatile("" ::: "memory");
#pragma unroll
    for (int pj = 0; pj < 4; ++pj) if (bad[pj]) v[pj] = ld16(rs, off + pj * pstride);
  }
  return pk_bf((g_val(v[0].x) + g_val(v[1].x)) + (g_val(v[2].x) + g_val(v[3].x)), (g_val(v[0].z) + g_val(v[1].z)) + (g_val(v[2].z) + g_val(v[3].z)));
}
DI Tag make_tag(u64 E) {
  const u64 T = E + 1;
  Tag tg;
  tg.hi = __builtin_amdgcn_readfirstlane((unsigned)(T >> 8));
  tg.lo8 = __builtin_amdgcn_readfirstlane((unsigned)T & 0xffu);
  tg.t32 = __builtin_amdgcn_readfirstlane((unsigned)T);
  tg.top = __builtin_amdgcn_readfirstlane((unsigned)(T >> 32));
  tg.nz = tg.t32 | 1u;
  return tg;
}

DI v4u poll_pair(rsrc_t rs, unsigned off, const Tag& tg) {
  v4u v = ld16(rs, off);
  unsigned spins = 0;
  while (!__all(g_ok2(v, tg)) && ++spins < SPIN_CAP) { __builtin_amdgcn_s_sleep(1); asm volatile("" ::: "memory"); v = ld16(rs, off); }
  return v;
}

DI void final_reduce(const Params& p, rsrc_t rs, const Tag& tg, int which, int a, int lane) {
  const int b = a >> 6, n0 = (a & 63) * 8, head = lane >> 1, hf = lane & 1;
  const unsigned off = WS_Y + ((((unsigned)which * 32 + head) * 2 + b) * 512 + n0 + 4 * hf) * 8;
  v4u v0 = ld16(rs, off), v1 = ld16(rs, off + 16);
  unsigned spins = 0;
  while (!(g_ok2(v0, tg) && g_ok2(v1, tg)) && ++spins < SPIN_CAP) { __builtin_amdgcn_s_sleep(1); asm volatile("" ::: "memory"); v0 = ld16(rs, off); v1 = ld16(rs, off + 16); }
  float r0 = swap32_add(g_val(v0.x), g_val(v1.x)), r1 = swap32_add(g_val(v0.z), g_val(v1.z));
  float x = swap16_add(r0, r1);
  x += dpp<0x4E>(x);
  x += dpp<0x124>(x);
  x += dpp<0x128>(x);
  if ((lane & 15) < 2) p.out[b * 1024 + which * 512 + n0 + 4 * hf + (lane >> 4)] = (u16)f2bf_bits(x);
}

DI float score4(const v4u& k, const v4u& q) {
  float d = dot2(k.x, q.x, 0.f); d = dot2(k.y, q.y, d); d = dot2(k.z, q.z, d); d = dot2(k.w, q.w, d);
  return rbf(rbf(row16_sum(d)) * SCALE);
}
template <int N>
DI void pv_record(rsrc_t rs, const Tag& tg, const float (&s)[N], const v4u (&V)[N], float m, unsigned ST, unsigned mg, unsigned rec, int tid) {
  const int lane = tid & 63, w = __builtin_amdgcn_readfirstlane(tid >> 6), c16 = lane & 15, g = lane >> 4;
  m = xrow_max(m);
  float l = 0.f, acc[8];
#pragma unroll
  for (int k = 0; k < 8; ++k) acc[k] = 0.f;
  const float mneg = -m * LOG2E;
#pragma unroll
  for (int i = 0; i < N; ++i) {
    const float pp = __builtin_amdgcn_exp2f(fmaf(s[i], LOG2E, mneg));
    l += pp;
#pragma unroll
    for (int k = 0; k < 4; ++k) { acc[2 * k] = fmaf(pp, bflo(V[i][k]), acc[2 * k]); acc[2 * k + 1] = fmaf(pp, bfhi(V[i][k]), acc[2 * k + 1]); }
  }
  l = xrow_sum(l);
  float r[4], t[2];
#pragma unroll
  for (int k = 0; k < 4; ++k) r[k] = swap32_add(acc[k], acc[k + 4]);
#pragma unroll
  for (int k = 0; k < 2; ++k) t[k] = swap16_add(r[k], r[k + 2]);
  lds_st<v2u>(mg + w * 512 + (8 * c16 + 4 * (g >> 1) + 2 * (g & 1)) * 4, v2u{__float_as_uint(t[0]), __float_as_uint(t[1])});
  if (lane == 0) lds_st<v2u>(ST + w * 8, v2u{__float_as_uint(m), __float_as_uint(l)});
  wg_barrier();
  if (tid < 192) {
    const f32x4 s0 = lds_ld<f32x4>(ST), s1 = lds_ld<f32x4>(ST + 16), s2 = lds_ld<f32x4>(ST + 32), s3 = lds_ld<f32x4>(ST + 48);
    const float M = fmaxf(fmaxf(fmaxf(s0[0], s0[2]), fmaxf(s1[0], s1[2])), fmaxf(fmaxf(s2[0], s2[2]), fmaxf(s3[0], s3[2])));
    float e[8];
    e[0] = __builtin_amdgcn_exp2f((s0[0] - M) * LOG2E); e[1] = __builtin_amdgcn_exp2f((s0[2] - M) * LOG2E);
    e[2] = __builtin_amdgcn_exp2f((s1[0] - M) * LOG2E); e[3] = __builtin_amdgcn_exp2f((s1[2] - M) * LOG2E);
    e[4] = __builtin_amdgcn_exp2f((s2[0] - M) * LOG2E); e[5] = __builtin_amdgcn_exp2f((s2[2] - M) * LOG2E);
    e[6] = __builtin_amdgcn_exp2f((s3[0] - M) * LOG2E); e[7] = __builtin_amdgcn_exp2f((s3[2] - M) * LOG2E);
    if (tid < 128) {
      float o = 0.f;
#pragma unroll
      for (int ww = 0; ww < 8; ++ww) o = fmaf(e[ww], lds_ld<float>(mg + ww * 512 + tid * 4), o);
      st8(rs, rec + tid * 8, g_lo(o, tg), tg.hi);
    } else if (tid == 128) {
      const float L = e[0] * s0[1] + e[1] * s0[3] + e[2] * s1[1] + e[3] * s1[3] + e[4] * s2[1] + e[5] * s2[3] + e[6] * s3[1] + e[7] * s3[3];
      st16(rs, rec + 1024, v4u{g_lo(L, tg), tg.hi, g_lo(M, tg), tg.hi});
    }
  }
}

DI void stream_wg(const Params& p, rsrc_t rs, const u64 E, int h, int j, unsigned lb) {
  const int b = j >> 1, half = j & 1, bh = b * 32 + h;
  const int tid = threadIdx.x, lane = tid & 63, w = __builtin_amdgcn_readfirstlane(tid >> 6), c16 = lane & 15, g = lane >> 4;
  const size_t off = ((size_t)bh * 8192 + 6144 + half * 1024 + w * (4 * NY)) * 128;
  const rsrc_t rk = mk_rsrc(p.kc + off, NY * 1024), rv = mk_rsrc(p.vc + off, NY * 1024);
  dma_slice(p.wqB + (size_t)(128 * j) * 4096 + h * 128, 4096, lb + 32768, w, lane);
  bf16x8 xa[4];
#pragma unroll
  for (int s = 0; s < 4; ++s) xa[s] = *reinterpret_cast<const bf16x8*>(p.xB + (lane & 1) * 512 + 128 * j + 32 * s + 8 * g);
  v4u K[NY], V[NY];
#pragma unroll
  for (int i = 0; i < KINIT; ++i) K[i] = ldnt_b(rk, lane * 16, i * 1024);
  asm volatile("s_waitcnt vmcnt(%0)" :: "i"(KINIT) : "memory");
  wg_barrier();
  const Tag tg = make_tag(E);
  {
    const f32x4v d = gemv_mfma(xa, lb + 32768, w, lane);
    gemv_publish(rs, tg, d, WS_Q1 + (((unsigned)h * 4 + j) * 2 + 1) * 2048, 128, w, lane);
  }
  STAMP(1);
  v4u qa[4];
  const unsigned qoff = WS_Q1 + ((((unsigned)h * 4) * 2 + 1) * 2 + b) * 1024 + lane * 16;
#pragma unroll
  for (int i = KINIT; i < 2 * NY; ++i) {
    if (i < NY) K[i] = ldnt_b(rk, lane * 16, i * 1024); else V[i - NY] = ldnt_b(rv, lane * 16, (i - NY) * 1024);
    if (i == NY - 4) { if (w == 0) {
#pragma unroll
        for (int pj = 0; pj < 4; ++pj) qa[pj] = ld16(rs, qoff + pj * 4096); } }
    if (i == NY + 4) {
      if (w == 0) {
        unsigned spins = 0;
        while (!__all(g_ok2(qa[0], tg) && g_ok2(qa[1], tg) && g_ok2(qa[2], tg) && g_ok2(qa[3], tg)) && ++spins < SPIN_CAP) {
          asm volatile("" ::: "memory");
#pragma unroll
          for (int pj = 0; pj < 4; ++pj) qa[pj] = ld16(rs, qoff + pj * 4096);
        }
        const float q0 = (g_val(qa[0].x) + g_val(qa[1].x)) + (g_val(qa[2].x) + g_val(qa[3].x));
        const float q1 = (g_val(qa[0].z) + g_val(qa[1].z)) + (g_val(qa[2].z) + g_val(qa[3].z));
        lds_st<unsigned>(lb + lane * 4, pk_bf(q0, q1));
      }
    }
    if (i < KINIT + KSLOW) asm volatile("s_waitcnt vmcnt(%0)" :: "i"(SWIN) : "memory");
    else if (i >= KWIN) asm volatile("s_waitcnt vmcnt(%0)" :: "i"(KWIN) : "memory");
  }
  dma_slice(p.wuv + (size_t)(128 * j) * 4096 + h * 128, 4096, lb + 65536, w, lane);
  wg_barrier();
  STAMP(2);
  {
    const v4u q = lds_ld<v4u>(lb + c16 * 16);
    float s[NY], m = -INFINITY;
#pragma unroll
    for (int i = 0; i < NY; ++i) { s[i] = score4(K[i], q); m = fmaxf(m, s[i]); }
    dma_slice(p.woA + (size_t)(h * 128) * 512 + 128 * j, 512, lb + 98304, w, lane);
    STAMP(3);
    pv_record<NY>(rs, tg, s, V, m, lb + 512, lb + 1024, WS_REC + (((unsigned)h * 4 + j) * 2 + 0) * 1088, tid);
  }
  STAMP(4);
  const unsigned MLS = lb + 6144, V16 = lb + 7168, R2 = lb + 8192;
  if (tid < 128) {
    const v4u v = poll_pair(rs, WS_ML + ((unsigned)h * 128 + tid) * 16, tg);
    lds_st<v2u>(MLS + tid * 8, v2u{v.x << 8, v.z << 8});
  }
  wg_barrier();
  STAMP(5);
  {
    v4u pv[4];
#pragma unroll
    for (int i = 0; i < 4; ++i) pv[i] = ld16(rs, WS_PART + (((unsigned)(16 * w + 4 * i + g) * 32 + h) * 512 + 128 * j + 8 * c16) * 2);
    const int bb = w >> 2;
    const v2u ml = lds_ld<v2u>(MLS + (bb * 64 + lane) * 8);
    const float m = __uint_as_float(ml.x), l = __uint_as_float(ml.y);
    float M = m;
    M = fmaxf(M, dpp<0xB1>(M)); M = fmaxf(M, dpp<0x4E>(M)); M = fmaxf(M, dpp<0x141>(M)); M = fmaxf(M, dpp<0x140>(M)); M = xrow_max(M);
    const float e = __builtin_amdgcn_exp2f((m - M) * LOG2E);
    const float L = xrow_sum(row16_sum(e * l));
    const float wt = e / L;
    float wi[4];
#pragma unroll
    for (int i = 0; i < 4; ++i) wi[i] = __shfl(wt, (16 * w + 4 * i + g) & 63);
    float acc[8];
#pragma unroll
    for (int k = 0; k < 8; ++k) acc[k] = 0.f;
#pragma unroll
    for (int i = 0; i < 4; ++i) {
#pragma unroll
      for (int k = 0; k < 4; ++k) { acc[2 * k] = fmaf(wi[i], bflo(pv[i][k]), acc[2 * k]); acc[2 * k + 1] = fmaf(wi[i], bfhi(pv[i][k]), acc[2 * k + 1]); }
    }
    float r[4], t[2];
#pragma unroll
    for (int k = 0; k < 4; ++k) r[k] = swap32_add(acc[k], acc[k + 4]);
#pragma unroll
    for (int k = 0; k < 2; ++k) t[k] = swap16_add(r[k], r[k + 2]);
    lds_st<v2u>(R2 + (w * 128 + 8 * c16 + 4 * (g >> 1) + 2 * (g & 1)) * 4, v2u{__float_as_uint(t[0]), __float_as_uint(t[1])});
  }
  wg_barrier();
  if (tid < 128) {
    const int bb = tid >> 6, cp = tid & 63;
    float x0 = 0.f, x1 = 0.f;
#pragma unroll
    for (int ww = 0; ww < 4; ++ww) { const v2u t = lds_ld<v2u>(R2 + ((4 * bb + ww) * 128 + 2 * cp) * 4); x0 += __uint_as_float(t.x); x1 += __uint_as_float(t.y); }
    lds_st<unsigned>(V16 + tid * 4, pk_bf(x0, x1));
  }
  wg_barrier();
  {
    bf16x8 f[4];
    vec_frags(f, V16, lane);
    const f32x4v d = gemv_mfma(f, lb + 65536, w, lane);
    gemv_publish(rs, tg, d, WS_C4 + ((unsigned)h * 4 + j) * 2048, 128, w, lane);
  }
  STAMP(6);
  if (tid < 128) {
    const unsigned v = gather4(rs, tg, WS_C4 + ((unsigned)h * 4) * 2048 + (tid >> 6) * 1024 + (tid & 63) * 16, 2048);
    lds_st<unsigned>(V16 + 512 + tid * 4, v);
  }
  wg_barrier();
  {
    bf16x8 f[4];
    vec_frags(f, V16 + 512, lane);
    const f32x4v d = gemv_mfma(f, lb + 98304, w, lane);
    gemv_publish(rs, tg, d, WS_Y + (((unsigned)0 * 32 + h) * 1024 + 128 * j) * 8, 512, w, lane);
  }
  STAMP(7);
  if (w == 0) final_reduce(p, rs, tg, 0, h * 4 + j, lane);
  STAMP(8);
}

DI void chain_wg(const Params& p, rsrc_t rs, const u64 E, int a, unsigned lb) {
  const int h = a >> 2, j = a & 3;
  const int tid = threadIdx.x, lane = tid & 63, w = __builtin_amdgcn_readfirstlane(tid >> 6), g = lane >> 4, c16 = lane & 15;
  const unsigned scr = lb + SCR;
  dma_slice(p.wqA + (size_t)(128 * j) * 4096 + h * 128, 4096, scr, w, lane);
  bf16x8 fa[4], WK[4];
#pragma unroll
  for (int s = 0; s < 4; ++s) fa[s] = *reinterpret_cast<const bf16x8*>(p.xA + (lane & 1) * 512 + 128 * j + 32 * s + 8 * g);
  {
    const rsrc_t rw = mk_rsrc(p.wuk + (size_t)(128 * j + 16 * w) * 4096 + h * 128, 16 * 8192);
#pragma unroll
    for (int s = 0; s < 4; ++s) WK[s] = __builtin_bit_cast(bf16x8, ldnt_b(rw, c16 * 8192 + g * 16, 64 * s));
  }
  const int bA = a >> 6;
  asm volatile("s_barrier" ::: "memory");
  dma_rows1k<0, CK0>(p.ckv + (size_t)a * 128 * 512, lb, w, lane);
  asm volatile("s_waitcnt vmcnt(%0)" :: "i"(4 + CK0) : "memory");
  wg_barrier();
  const Tag tg = make_tag(E);
  {
    const f32x4v d = gemv_mfma(fa, scr, w, lane);
    gemv_publish(rs, tg, d, WS_Q1 + (((unsigned)h * 4 + j) * 2 + 0) * 2048, 128, w, lane);
  }
  STAMP(1);
  dma_rows1k<CK0, CK1>(p.ckv + (size_t)a * 128 * 512, lb, w, lane);
  wg_barrier();
  if (tid < 128) {
    const unsigned v = gather4(rs, tg, WS_Q1 + (((unsigned)h * 4) * 4 + (tid >> 6)) * 1024 + (tid & 63) * 16, 4096);
    lds_st<unsigned>(scr + tid * 4, v);
  } else if (tid < 192) {
    const unsigned v = gather4(rs, tg, WS_Q1 + (((unsigned)h * 4) * 4 + 2 + (j >> 1)) * 1024 + lane * 16, 4096);
    lds_st<unsigned>(scr + 512 + lane * 4, v);
  }
  wg_barrier();
  STAMP(2);
  const v4u qB = lds_ld<v4u>(scr + 512 + c16 * 16);
  {
    bf16x8 qf[4];
    vec_frags(qf, scr, lane);
    f32x4v d = {0.f, 0.f, 0.f, 0.f};
#pragma unroll
    for (int s = 0; s < 4; ++s) d = __builtin_amdgcn_mfma_f32_16x16x32_bf16(WK[s], qf[s], d, 0, 0, 0);
    if (c16 < 2)
      st16(rs, WS_QL + (((unsigned)c16 * 32 + h) * 256 + 64 * j + 8 * w + 2 * g) * 8, v4u{pk_bf(d[0], d[1]), tg.t32, pk_bf(d[2], d[3]), tg.t32});
  }
  STAMP(3);
  dma_rows1k<CK1, 16>(p.ckv + (size_t)a * 128 * 512, lb, w, lane);
  const int r32 = lane & 31, hh = lane >> 5;
  float sc[8];
  s16x4 xt[32];
  bf16x8 ka[16];
  const unsigned fr = swz(r32);
  {
    v4u v[8];
    const unsigned base = WS_QL + (unsigned)bA * 65536 + tid * 16;
#pragma unroll
    for (int i = 0; i < 8; ++i) { v[i] = ld16(rs, base + i * 8192); }
    asm volatile("s_waitcnt vmcnt(8)" ::: "memory");
    wg_barrier();
    STAMP(11);
    {
      const unsigned abase = lb + (32 * (w & 3) + r32) * 1024 + 512 * (w >> 2);
#pragma unroll
      for (int s = 0; s < 16; ++s) ka[s] = lds_ld<bf16x8>(abase + 16 * ((unsigned)((2 * s + hh) & 15) ^ fr) + 256 * (s >> 3));
      const unsigned blk = (lane >> 4) & 1, q = (lane & 15) >> 2, pp = lane & 3;
      unsigned tb[4];
#pragma unroll
      for (int mt = 0; mt < 2; ++mt)
#pragma unroll
        for (int t = 0; t < 2; ++t) {
          const unsigned row = 8 * hh + 4 * t + q, chn = 8 * w + 4 * mt + 2 * blk + (pp >> 1);
          tb[mt * 2 + t] = lb + row * 1024 + 16 * (chn ^ swz(row)) + 8 * (pp & 1);
        }
#pragma unroll
      for (int hf = 0; hf < 2; ++hf) {
        asm volatile(
            "ds_read_b64_tr_b16 %0, %16\n\tds_read_b64_tr_b16 %1, %17\n\tds_read_b64_tr_b16 %2, %18\n\tds_read_b64_tr_b16 %3, %19\n\t"
            "ds_read_b64_tr_b16 %4, %16 offset:16384\n\tds_read_b64_tr_b16 %5, %17 offset:16384\n\tds_read_b64_tr_b16 %6, %18 offset:16384\n\tds_read_b64_tr_b16 %7, %19 offset:16384\n\t"
            "ds_read_b64_tr_b16 %8, %16 offset:32768\n\tds_read_b64_tr_b16 %9, %17 offset:32768\n\tds_read_b64_tr_b16 %10, %18 offset:32768\n\tds_read_b64_tr_b16 %11, %19 offset:32768\n\t"
            "ds_read_b64_tr_b16 %12, %16 offset:49152\n\tds_read_b64_tr_b16 %13, %17 offset:49152\n\tds_read_b64_tr_b16 %14, %18 offset:49152\n\tds_read_b64_tr_b16 %15, %19 offset:49152\n\t"
            "s_waitcnt lgkmcnt(0)"
            : "=&v"(xt[16 * hf + 0]), "=&v"(xt[16 * hf + 1]), "=&v"(xt[16 * hf + 2]), "=&v"(xt[16 * hf + 3]),
              "=&v"(xt[16 * hf + 4]), "=&v"(xt[16 * hf + 5]), "=&v"(xt[16 * hf + 6]), "=&v"(xt[16 * hf + 7]),
              "=&v"(xt[16 * hf + 8]), "=&v"(xt[16 * hf + 9]), "=&v"(xt[16 * hf + 10]), "=&v"(xt[16 * hf + 11]),
              "=&v"(xt[16 * hf + 12]), "=&v"(xt[16 * hf + 13]), "=&v"(xt[16 * hf + 14]), "=&v"(xt[16 * hf + 15])
            : "v"(tb[0] + hf * 65536), "v"(tb[1] + hf * 65536), "v"(tb[2] + hf * 65536), "v"(tb[3] + hf * 65536) : "memory");
      }
    }
    STAMP(12);
    unsigned spins = 0;
    const unsigned cq = tid & 127, h0 = tid >> 7;
    for (;;) {
      bool bad[8], any = false;
#pragma unroll
      for (int i = 0; i < 8; ++i) { bad[i] = !__all(v[i].y == tg.t32 && v[i].w == tg.t32); any = any || bad[i]; }
      if (!any || ++spins >= SPIN_CAP) break;
      asm volatile("" ::: "memory");
#pragma unroll
      for (int i = 0; i < 8; ++i) if (bad[i]) v[i] = ld16(rs, base + i * 8192);
    }
#pragma unroll
    for (int i = 0; i < 8; ++i) {
      const unsigned head = 4 * i + h0;
      lds_st<v2u>(scr + head * 1024 + 16 * ((cq >> 1) ^ swz(head)) + 8 * (cq & 1), v2u{v[i].x, v[i].z});
    }
  }
  wg_barrier();
  STAMP(4);
  const size_t koff = ((size_t)((j >> 1) * 32 + h) * 8192 + 6144 + (j & 1) * 1024 + 32 * NY + w * (4 * NX)) * 128;
  const rsrc_t rk = mk_rsrc(p.kc + koff, NX * 1024), rv = mk_rsrc(p.vc + koff, NX * 1024);
  v4u K[NX], V[NX];
#pragma unroll
  for (int i = 0; i < 7; ++i) K[i] = ldnt_b(rk, lane * 16, i * 1024);
  {
    const int ch = w >> 2;
    f32x16 acc;
#pragma unroll
    for (int i = 0; i < 16; ++i) acc[i] = 0.f;
    const unsigned bbase = scr + r32 * 1024 + 512 * ch;
#pragma unroll
    for (int s = 0; s < 16; ++s) {
      const bf16x8 bv = lds_ld<bf16x8>(bbase + 16 * ((unsigned)((2 * s + hh) & 15) ^ fr) + 256 * (s >> 3));
      acc = __builtin_amdgcn_mfma_f32_32x32x16_bf16(ka[s], bv, acc, 0, 0, 0);
      if ((s & 7) == 7) __builtin_amdgcn_sched_barrier(0);
    }
    f32x4 s0, s1;
#pragma unroll
    for (int k = 0; k < 4; ++k) { s0[k] = ch ? acc[k] : acc[8 + k]; s1[k] = ch ? acc[4 + k] : acc[12 + k]; }
    const unsigned XB = lb + 32768;
    lds_st<f32x4>(XB + w * 2048 + lane * 16, s0);
    lds_st<f32x4>(XB + w * 2048 + 1024 + lane * 16, s1);
    wg_barrier();
    const f32x4 t0 = lds_ld<f32x4>(XB + (w ^ 4) * 2048 + lane * 16), t1 = lds_ld<f32x4>(XB + (w ^ 4) * 2048 + 1024 + lane * 16);
#pragma unroll
    for (int k = 0; k < 4; ++k) { sc[k] = ((ch ? acc[8 + k] : acc[k]) + t0[k]) * SCALE; sc[4 + k] = ((ch ? acc[12 + k] : acc[4 + k]) + t1[k]) * SCALE; }
  }
#pragma unroll
  for (int i = 7; i < NX; ++i) K[i] = ldnt_b(rk, lane * 16, i * 1024);
  const unsigned MX = scr + 16384, LS = scr + 17408, PB = scr + 18432;
  float Mh;
  {
    float m = sc[0];
#pragma unroll
    for (int k = 1; k < 8; ++k) m = fmaxf(m, sc[k]);
    { auto t = __builtin_amdgcn_permlane32_swap(__float_as_uint(m), __float_as_uint(m), false, false); m = fmaxf(__uint_as_float(t[0]), __uint_as_float(t[1])); }
    if (lane < 32) lds_st<float>(MX + (lane * 8 + w) * 4, m);
    wg_barrier();
    const f32x4 ma = lds_ld<f32x4>(MX + r32 * 32), mb = lds_ld<f32x4>(MX + r32 * 32 + 16);
    float M = fmaxf(fmaxf(fmaxf(ma[0], ma[1]), fmaxf(ma[2], ma[3])), fmaxf(fmaxf(mb[0], mb[1]), fmaxf(mb[2], mb[3])));
    M = round24(M);
    Mh = M;
    const float mneg = -M * LOG2E;
    unsigned pk[4]; float l = 0.f;
#pragma unroll
    for (int k = 0; k < 4; ++k) {
      const float p0 = __builtin_amdgcn_exp2f(fmaf(sc[2 * k], LOG2E, mneg)), p1 = __builtin_amdgcn_exp2f(fmaf(sc[2 * k + 1], LOG2E, mneg));
      pk[k] = pk_bf(p0, p1);
      l += bflo(pk[k]) + bfhi(pk[k]);
    }
    { auto t = __builtin_amdgcn_permlane32_swap(__float_as_uint(l), __float_as_uint(l), false, false); l = __uint_as_float(t[0]) + __uint_as_float(t[1]); }
    if (lane < 32) lds_st<float>(LS + (lane * 8 + w) * 4, l);
    const int kt = w & 3, ch = w >> 2;
    lds_st<v2u>(PB + r32 * 272 + (32 * kt + 16 * ch + 4 * hh) * 2, v2u{pk[0], pk[1]});
    lds_st<v2u>(PB + r32 * 272 + (32 * kt + 16 * ch + 8 + 4 * hh) * 2, v2u{pk[2], pk[3]});
  }
  wg_barrier();
  f32x16 o0, o1;
#pragma unroll
  for (int i = 0; i < 16; ++i) { o0[i] = 0.f; o1[i] = 0.f; }
#pragma unroll
  for (int s = 0; s < 8; ++s) {
    const bf16x8 pv = lds_ld<bf16x8>(PB + r32 * 272 + 32 * s + 16 * hh);
    o0 = __builtin_amdgcn_mfma_f32_32x32x16_bf16(__builtin_shufflevector(xt[4 * s], xt[4 * s + 1], 0, 1, 2, 3, 4, 5, 6, 7), pv, o0, 0, 0, 0);
    o1 = __builtin_amdgcn_mfma_f32_32x32x16_bf16(__builtin_shufflevector(xt[4 * s + 2], xt[4 * s + 3], 0, 1, 2, 3, 4, 5, 6, 7), pv, o1, 0, 0, 0);
  }
  STAMP(5);
  {
    const unsigned tb = lb + 32768 + w * 4096;
#pragma unroll
    for (int gq = 0; gq < 4; ++gq) {
      lds_st<v2u>(tb + r32 * 128 + 16 * (gq ^ (r32 & 7)) + 8 * hh, v2u{pk_bf(o0[4 * gq], o0[4 * gq + 1]), pk_bf(o0[4 * gq + 2], o0[4 * gq + 3])});
      lds_st<v2u>(tb + r32 * 128 + 16 * ((4 + gq) ^ (r32 & 7)) + 8 * hh, v2u{pk_bf(o1[4 * gq], o1[4 * gq + 1]), pk_bf(o1[4 * gq + 2], o1[4 * gq + 3])});
    }
    asm volatile("s_waitcnt lgkmcnt(0)" ::: "memory");
#pragma unroll
    for (int i = 0; i < 4; ++i) {
      const int head = 8 * i + (lane >> 3);
      const v4u v = lds_ld<v4u>(tb + head * 128 + 16 * ((lane & 7) ^ (head & 7)));
      st16(rs, WS_PART + (((unsigned)a * 32 + head) * 512 + 64 * w + 8 * (lane & 7)) * 2, v);
    }
    asm volatile("" ::: "memory");
#pragma unroll
    for (int i = 0; i < 6; ++i) V[i] = ldnt_b(rv, lane * 16, i * 1024);
    asm volatile("s_waitcnt vmcnt(6)" ::: "memory");
  }
  wg_barrier();
  if (w == 0 && lane < 32) {
    const f32x4 la = lds_ld<f32x4>(LS + lane * 32), lc = lds_ld<f32x4>(LS + lane * 32 + 16);
    const float l = ((la[0] + la[1]) + (la[2] + la[3])) + ((lc[0] + lc[1]) + (lc[2] + lc[3]));
    st16(rs, WS_ML + ((unsigned)lane * 128 + a) * 16, v4u{g_lo(Mh, tg), tg.hi, g_lo(l, tg), tg.hi});
  }
  STAMP(6);
  if (a == 0 && w == 7) {
    v4u d0 = ld16(rs, WS_Q1 + ((unsigned)lane * 2 + 1) * 2048), d1 = ld16(rs, WS_Q1 + ((unsigned)(lane + 64) * 2 + 1) * 2048);
    unsigned spins = 0;
    while (!__all(g_ok2(d0, tg) && g_ok2(d1, tg)) && ++spins < SPIN_CAP) {
      __builtin_amdgcn_s_sleep(2); asm volatile("" ::: "memory");
      d0 = ld16(rs, WS_Q1 + ((unsigned)lane * 2 + 1) * 2048); d1 = ld16(rs, WS_Q1 + ((unsigned)(lane + 64) * 2 + 1) * 2048);
    }
    if (lane == 0) __hip_atomic_store(reinterpret_cast<u64*>(p.ws + WS_EPOCH), ((u64)tg.top << 32) | tg.t32, __ATOMIC_RELAXED, __HIP_MEMORY_SCOPE_AGENT);
  }
#pragma unroll
  for (int i = 6; i < NX; ++i) V[i] = ldnt_b(rv, lane * 16, i * 1024);
  {
    float s[NX], m = -INFINITY;
#pragma unroll
    for (int i = 0; i < NX; ++i) { s[i] = score4(K[i], qB); m = fmaxf(m, s[i]); }
    dma_slice(p.woB + (size_t)(h * 128) * 512 + 128 * j, 512, lb, w, lane);
    STAMP(7);
    pv_record<NX>(rs, tg, s, V, m, lb + 65536, lb + 66560, WS_REC + (((unsigned)h * 4 + j) * 2 + 1) * 1088, tid);
  }
  STAMP(8);
  const unsigned V16 = lb + 71680;
  if (tid < 128) {
    const int bb = tid >> 6, dp = tid & 63;
    const unsigned r0 = WS_REC + (((unsigned)h * 4 + 2 * bb) * 2) * 1088;
    v4u o[4], st[4];
#pragma unroll
    for (int r = 0; r < 4; ++r) { o[r] = ld16(rs, r0 + r * 1088 + dp * 16); st[r] = ld16(rs, r0 + r * 1088 + 1024); }
    unsigned spins = 0;
    for (;;) {
      bool bad[4], any = false;
#pragma unroll
      for (int r = 0; r < 4; ++r) { bad[r] = !__all(g_ok2(o[r], tg) && g_ok2(st[r], tg)); any = any || bad[r]; }
      if (!any || ++spins >= SPIN_CAP) break;
      asm volatile("" ::: "memory");
#pragma unroll
      for (int r = 0; r < 4; ++r) if (bad[r]) { o[r] = ld16(rs, r0 + r * 1088 + dp * 16); st[r] = ld16(rs, r0 + r * 1088 + 1024); }
    }
    const float Mx = fmaxf(fmaxf(g_val(st[0].z), g_val(st[1].z)), fmaxf(g_val(st[2].z), g_val(st[3].z)));
    float L = 0.f, o0 = 0.f, o1 = 0.f;
#pragma unroll
    for (int r = 0; r < 4; ++r) {
      const float e = __builtin_amdgcn_exp2f((g_val(st[r].z) - Mx) * LOG2E);
      L = fmaf(e, g_val(st[r].x), L); o0 = fmaf(e, g_val(o[r].x), o0); o1 = fmaf(e, g_val(o[r].z), o1);
    }
    const float inv = 1.f / L;
    lds_st<unsigned>(V16 + tid * 4, pk_bf(o0 * inv, o1 * inv));
  }
  asm volatile("s_waitcnt vmcnt(0)" ::: "memory");
  wg_barrier();
  STAMP(9);
  {
    bf16x8 f[4];
    vec_frags(f, V16, lane);
    const f32x4v d = gemv_mfma(f, lb, w, lane);
    gemv_publish(rs, tg, d, WS_Y + (((unsigned)1 * 32 + h) * 1024 + 128 * j) * 8, 512, w, lane);
  }
  if (w == 0) final_reduce(p, rs, tg, 1, a, lane);
  STAMP(10);
}

__global__ void __launch_bounds__(512, 2) fused_decode_kernel(Params p) {
  extern __shared__ __attribute__((aligned(16))) char smem[];
  const unsigned lb = (unsigned)(uintptr_t)(LAS char*)smem;
  asm volatile("" :: "s"(p.xA), "s"(p.wqA), "s"(p.wqB), "s"(p.ws));
  STAMP(0);
  u64 pc; asm volatile("s_getpc_b64 %0" : "=s"(pc));
  const rsrc_t rs = __builtin_amdgcn_make_buffer_rsrc(p.ws, 0, WS_END, 0x00020000);
  const u64 E = __hip_atomic_load(reinterpret_cast<u64*>(p.ws + WS_EPOCH), __ATOMIC_RELAXED, __HIP_MEMORY_SCOPE_AGENT);
  const int gidx = blockIdx.x;
  const int role = (gidx >> 3) & 1, k = gidx >> 4, head = 4 * (gidx & 7) + (k >> 2), j = k & 3;
  if (role == 0) {
    const unsigned o = (threadIdx.x * 64u < CODE_BYTES) ? threadIdx.x * 64u : 0u;
    const u64 ad = (pc & ~63ull) + o;
    unsigned keep;
    asm volatile("s_mov_b32 %0, m0\n\ts_mov_b32 m0, %2\n\ts_nop 0\n\tglobal_load_lds_dword %1, off\n\ts_mov_b32 m0, %0"
                 : "=&s"(keep) : "v"(ad), "s"(lb + 16384u + (unsigned)__builtin_amdgcn_readfirstlane(threadIdx.x >> 6) * 256u) : "memory");
    stream_wg(p, rs, E, head, j, lb);
  }
  else chain_wg(p, rs, E, head * 4 + j, lb);
}

extern "C" void kernel_launch(void* const* d_in, const int* in_sizes, int n_in, void* d_out, int out_size, void* d_ws, size_t ws_size,
                              hipStream_t stream) {
  Params p;
  p.xA = (const u16*)d_in[0]; p.ckv = (const u16*)d_in[1]; p.wqA = (const u16*)d_in[2]; p.wuk = (const u16*)d_in[3];
  p.wuv = (const u16*)d_in[4]; p.woA = (const u16*)d_in[5]; p.xB = (const u16*)d_in[6]; p.kc = (const u16*)d_in[7];
  p.vc = (const u16*)d_in[8]; p.wqB = (const u16*)d_in[9]; p.woB = (const u16*)d_in[10];
  p.out = (u16*)d_out; p.ws = (unsigned char*)d_ws;
  (void)hipFuncSetAttribute((const void*)fused_decode_kernel, hipFuncAttributeMaxDynamicSharedMemorySize, LDS_BYTES);
  fused_decode_kernel<<<256, 512, LDS_BYTES, stream>>>(p);
}
```

```cpp
#include <hip/hip_runtime.h>
#include <stdint.h>

typedef unsigned short u16;
typedef unsigned long long u64;
typedef unsigned v4u __attribute__((ext_vector_type(4)));
typedef unsigned v2u __attribute__((ext_vector_type(2)));
typedef float f32x4 __attribute__((ext_vector_type(4)));
typedef float f32x16 __attribute__((ext_vector_type(16)));
typedef short bf16x8 __attribute__((ext_vector_type(8)));
typedef short s16x4 __attribute__((ext_vector_type(4)));
typedef __bf16 bf16x2 __attribute__((ext_vector_type(2)));
#define LAS __attribute__((address_space(3)))
#define DI __device__ __forceinline__

constexpr unsigned WS_EPOCH = 0;
constexpr unsigned WS_Q1 = 4096;
constexpr unsigned WS_QL = WS_Q1 + 65536u * 8;
constexpr unsigned WS_ML = WS_QL + 16384u * 8;
constexpr unsigned WS_C4 = WS_ML + 8192u * 8;
constexpr unsigned WS_REC = WS_C4 + 32768u * 8;
constexpr unsigned WS_Y = WS_REC + 32u * 8 * 136 * 8;
constexpr unsigned WS_PART = WS_Y + 65536u * 8;
constexpr unsigned WS_END = WS_PART + 128u * 32 * 512 * 2;

constexpr unsigned CODE_BYTES = 16384;
constexpr int LDS_BYTES = 163840;
constexpr unsigned SCR = 131072;
constexpr unsigned SPIN_CAP = 1u << 16;
constexpr int NY = 20, NX = 32 - NY;
constexpr int KINIT = 1;
constexpr int KSLOW = 3, SWIN = 0;
constexpr int KWIN = 8;
constexpr int CK0 = 6, CK1 = 12;
constexpr float SCALE = 0.08838834764831845f;
constexpr float LOG2E = 1.4426950408889634f;

struct Params {
  const u16 *xA, *ckv, *wqA, *wuk, *wuv, *woA, *xB, *kc, *vc, *wqB, *woB;
  u16* out;
  unsigned char* ws;
};

DI float bflo(unsigned w) { return __uint_as_float(w << 16); }
DI float bfhi(unsigned w) { return __uint_as_float(w & 0xffff0000u); }
DI unsigned f2bf_bits(float f) { return (unsigned)__builtin_bit_cast(unsigned short, (__bf16)f); }
DI float rbf(float f) { return __uint_as_float(f2bf_bits(f) << 16); }
DI unsigned pk_bf(float lo, float hi) { return f2bf_bits(lo) | (f2bf_bits(hi) << 16); }
DI float dot2(unsigned a, unsigned b, float acc) {
  return __builtin_amdgcn_fdot2_f32_bf16(__builtin_bit_cast(bf16x2, a), __builtin_bit_cast(bf16x2, b), acc, false);
}
template <int CTRL> DI float dpp(float x) {
  return __builtin_bit_cast(float, __builtin_amdgcn_mov_dpp(__builtin_bit_cast(int, x), CTRL, 0xf, 0xf, true));
}
DI float row16_sum(float x) { x += dpp<0xB1>(x); x += dpp<0x4E>(x); x += dpp<0x141>(x); x += dpp<0x140>(x); return x; }
DI float xrow_max(float x) {
  auto s = __builtin_amdgcn_permlane16_swap(__float_as_uint(x), __float_as_uint(x), false, false);
  x = fmaxf(__uint_as_float(s[0]), __uint_as_float(s[1]));
  auto t = __builtin_amdgcn_permlane32_swap(__float_as_uint(x), __float_as_uint(x), false, false);
  return fmaxf(__uint_as_float(t[0]), __uint_as_float(t[1]));
}
DI float xrow_sum(float x) {
  auto s = __builtin_amdgcn_permlane16_swap(__float_as_uint(x), __float_as_uint(x), false, false);
  x = __uint_as_float(s[0]) + __uint_as_float(s[1]);
  auto t = __builtin_amdgcn_permlane32_swap(__float_as_uint(x), __float_as_uint(x), false, false);
  return __uint_as_float(t[0]) + __uint_as_float(t[1]);
}
DI float swap32_add(float a, float b) {
  auto t = __builtin_amdgcn_permlane32_swap(__float_as_uint(a), __float_as_uint(b), false, false);
  return __uint_as_float(t[0]) + __uint_as_float(t[1]);
}
DI float swap16_add(float a, float b) {
  auto t = __builtin_amdgcn_permlane16_swap(__float_as_uint(a), __float_as_uint(b), false, false);
  return __uint_as_float(t[0]) + __uint_as_float(t[1]);
}
DI void wg_barrier() { asm volatile("s_waitcnt lgkmcnt(0)\n\ts_barrier" ::: "memory"); }
DI void glds16_nt(const void* gsrc, unsigned lds_dst) {
  unsigned keep;
  asm volatile("s_mov_b32 %0, m0\n\ts_mov_b32 m0, %2\n\ts_nop 0\n\tglobal_load_lds_dwordx4 %1, off nt\n\ts_mov_b32 m0, %0"
               : "=&s"(keep) : "v"(gsrc), "s"(lds_dst) : "memory");
}
typedef __amdgpu_buffer_rsrc_t rsrc_t;
DI rsrc_t mk_rsrc(const void* base, unsigned bytes) {
  const u64 b = (u64)base;
  const unsigned lo = __builtin_amdgcn_readfirstlane((unsigned)b), hi = __builtin_amdgcn_readfirstlane((unsigned)(b >> 32));
  return __builtin_amdgcn_make_buffer_rsrc((void*)(((u64)hi << 32) | lo), 0, bytes, 0x00020000);
}
DI v4u ldnt_b(rsrc_t r, unsigned voff, unsigned soff) { return __builtin_amdgcn_raw_buffer_load_b128(r, voff, soff, 2); }
DI v4u ld16(rsrc_t r, unsigned off) { return __builtin_amdgcn_raw_buffer_load_b128(r, off, 0, 16); }
DI void st8(rsrc_t r, unsigned off, unsigned lo, unsigned hi) { v2u v = {lo, hi}; __builtin_amdgcn_raw_buffer_store_b64(v, r, off, 0, 16); }
DI void st16(rsrc_t r, unsigned off, v4u v) { __builtin_amdgcn_raw_buffer_store_b128(v, r, off, 0, 16); }

struct Tag { unsigned hi, lo8, t32, top; };
DI unsigned g_lo(float v, const Tag& t) { return ((__float_as_uint(v) + 0x80u) >> 8) | (t.lo8 << 24); }
DI float g_val(unsigned lo) { return __uint_as_float(lo << 8); }
DI bool g_ok(unsigned lo, unsigned hi, const Tag& t) { return hi == t.hi && (lo >> 24) == t.lo8; }
DI bool g_ok2(const v4u& v, const Tag& t) { return g_ok(v.x, v.y, t) && g_ok(v.z, v.w, t); }
DI float round24(float v) { return __uint_as_float((__float_as_uint(v) + 0x80u) & 0xffffff00u); }
DI unsigned swz(unsigned r) { return ((r & 3u) << 2) | ((r >> 2) & 3u); }

template <typename T> DI void lds_st(unsigned addr, T v) { *(LAS T*)(uintptr_t)addr = v; }
template <typename T> DI T lds_ld(unsigned addr) { return *(LAS T*)(uintptr_t)addr; }


typedef float f32x4v __attribute__((ext_vector_type(4)));

DI void dma_slice(const u16* src0, size_t row_stride, unsigned dst, int w, int lane) {
  asm volatile("" : "+v"(lane));
  const int g = lane >> 4, c16 = lane & 15;
  const u16* s = src0 + (size_t)(16 * w + g) * row_stride;
#pragma unroll
  for (int i = 0; i < 4; ++i) glds16_nt(s + (size_t)(4 * i) * row_stride + 8 * (c16 ^ ((g << 2) | i)), dst + (16 * w + 4 * i) * 256);
}
DI f32x4v gemv_mfma(const bf16x8 (&a)[4], unsigned wbase, int w, int lane) {
  const unsigned g = lane >> 4, q = (lane & 15) >> 2, pp = lane & 3;
  const unsigned chn = 2 * w + (pp >> 1);
  const unsigned a0 = wbase + (8 * g + q) * 256 + 16 * (chn ^ ((q << 2) | ((2 * g) & 3))) + 8 * (pp & 1);
  const unsigned a1 = wbase + (8 * g + 4 + q) * 256 + 16 * (chn ^ ((q << 2) | ((2 * g + 1) & 3))) + 8 * (pp & 1);
  s16x4 x[8];
  asm volatile("ds_read_b64_tr_b16 %0, %8\n\tds_read_b64_tr_b16 %1, %9\n\t"
               "ds_read_b64_tr_b16 %2, %8 offset:8192\n\tds_read_b64_tr_b16 %3, %9 offset:8192\n\t"
               "ds_read_b64_tr_b16 %4, %8 offset:16384\n\tds_read_b64_tr_b16 %5, %9 offset:16384\n\t"
               "ds_read_b64_tr_b16 %6, %8 offset:24576\n\tds_read_b64_tr_b16 %7, %9 offset:24576\n\ts_waitcnt lgkmcnt(0)"
               : "=&v"(x[0]), "=&v"(x[1]), "=&v"(x[2]), "=&v"(x[3]), "=&v"(x[4]), "=&v"(x[5]), "=&v"(x[6]), "=&v"(x[7])
               : "v"(a0), "v"(a1) : "memory");
  f32x4v d = {0.f, 0.f, 0.f, 0.f};
#pragma unroll
  for (int s = 0; s < 4; ++s)
    d = __builtin_amdgcn_mfma_f32_16x16x32_bf16(a[s], __builtin_shufflevector(x[2 * s], x[2 * s + 1], 0, 1, 2, 3, 4, 5, 6, 7), d, 0, 0, 0);
  return d;
}

template <int I0, int I1> DI void dma_rows1k(const u16* src, unsigned dst, int w, int lane) {
  asm volatile("" : "+v"(lane));
  const u16* s = src + (size_t)(16 * w) * 512;
#pragma unroll
  for (int i = I0; i < I1; ++i) glds16_nt(s + i * 512 + 8 * (lane ^ swz(i)), dst + (16 * w + i) * 1024);
}
DI void vec_frags(bf16x8 (&a)[4], unsigned vbase, int lane) {
  const unsigned ad = vbase + (lane & 1) * 256 + (lane >> 4) * 16;
#pragma unroll
  for (int s = 0; s < 4; ++s) a[s] = lds_ld<bf16x8>(ad + 64 * s);
}
DI void gemv_publish(rsrc_t rs, const Tag& tg, const f32x4v& d, unsigned base, unsigned bstride, int w, int lane) {
  if (lane < 32) {
    const float v = lane < 16 ? d[0] : d[1];
    st8(rs, base + ((lane >> 4) * bstride + 16 * w + (lane & 15)) * 8, g_lo(v, tg), tg.hi);
  }
}
DI unsigned gather4(rsrc_t rs, const Tag& tg, unsigned off, unsigned pstride) {
  v4u v[4];
#pragma unroll
  for (int pj = 0; pj < 4; ++pj) v[pj] = ld16(rs, off + pj * pstride);
  unsigned spins = 0;
  for (;;) {
    bool bad[4], any = false;
#pragma unroll
    for (int pj = 0; pj < 4; ++pj) { bad[pj] = !__all(g_ok2(v[pj], tg)); any = any || bad[pj]; }
    if (!any || ++spins >= SPIN_CAP) break;
    asm volatile("" ::: "memory");
#pragma unroll
    for (int pj = 0; pj < 4; ++pj) if (bad[pj]) v[pj] = ld16(rs, off + pj * pstride);
  }
  return pk_bf((g_val(v[0].x) + g_val(v[1].x)) + (g_val(v[2].x) + g_val(v[3].x)), (g_val(v[0].z) + g_val(v[1].z)) + (g_val(v[2].z) + g_val(v[3].z)));
}
DI Tag make_tag(u64 E) {
  const u64 T = E + 1;
  Tag tg;
  tg.hi = __builtin_amdgcn_readfirstlane((unsigned)(T >> 8));
  tg.lo8 = __builtin_amdgcn_readfirstlane((unsigned)T & 0xffu);
  tg.t32 = __builtin_amdgcn_readfirstlane((unsigned)T);
  tg.top = __builtin_amdgcn_readfirstlane((unsigned)(T >> 32));
  return tg;
}

DI v4u poll_pair(rsrc_t rs, unsigned off, const Tag& tg) {
  v4u v = ld16(rs, off);
  unsigned spins = 0;
  while (!__all(g_ok2(v, tg)) && ++spins < SPIN_CAP) { __builtin_amdgcn_s_sleep(1); asm volatile("" ::: "memory"); v = ld16(rs, off); }
  return v;
}

DI void final_reduce(const Params& p, rsrc_t rs, const Tag& tg, int which, int a, int lane) {
  const int b = a >> 6, n0 = (a & 63) * 8, head = lane >> 1, hf = lane & 1;
  const unsigned off = WS_Y + ((((unsigned)which * 32 + head) * 2 + b) * 512 + n0 + 4 * hf) * 8;
  v4u v0 = ld16(rs, off), v1 = ld16(rs, off + 16);
  unsigned spins = 0;
  while (!(g_ok2(v0, tg) && g_ok2(v1, tg)) && ++spins < SPIN_CAP) { __builtin_amdgcn_s_sleep(1); asm volatile("" ::: "memory"); v0 = ld16(rs, off); v1 = ld16(rs, off + 16); }
  float r0 = swap32_add(g_val(v0.x), g_val(v1.x)), r1 = swap32_add(g_val(v0.z), g_val(v1.z));
  float x = swap16_add(r0, r1);
  x += dpp<0x4E>(x);
  x += dpp<0x124>(x);
  x += dpp<0x128>(x);
  if ((lane & 15) < 2) p.out[b * 1024 + which * 512 + n0 + 4 * hf + (lane >> 4)] = (u16)f2bf_bits(x);
}

DI float score4(const v4u& k, const v4u& q) {
  float d = dot2(k.x, q.x, 0.f); d = dot2(k.y, q.y, d); d = dot2(k.z, q.z, d); d = dot2(k.w, q.w, d);
  return rbf(rbf(row16_sum(d)) * SCALE);
}
template <int N>
DI void pv_record(rsrc_t rs, const Tag& tg, const float (&s)[N], const v4u (&V)[N], float m, unsigned ST, unsigned mg, unsigned rec, int tid) {
  const int lane = tid & 63, w = __builtin_amdgcn_readfirstlane(tid >> 6), c16 = lane & 15, g = lane >> 4;
  m = xrow_max(m);
  float l = 0.f, acc[8];
#pragma unroll
  for (int k = 0; k < 8; ++k) acc[k] = 0.f;
  const float mneg = -m * LOG2E;
#pragma unroll
  for (int i = 0; i < N; ++i) {
    const float pp = __builtin_amdgcn_exp2f(fmaf(s[i], LOG2E, mneg));
    l += pp;
#pragma unroll
    for (int k = 0; k < 4; ++k) { acc[2 * k] = fmaf(pp, bflo(V[i][k]), acc[2 * k]); acc[2 * k + 1] = fmaf(pp, bfhi(V[i][k]), acc[2 * k + 1]); }
  }
  l = xrow_sum(l);
  float r[4], t[2];
#pragma unroll
  for (int k = 0; k < 4; ++k) r[k] = swap32_add(acc[k], acc[k + 4]);
#pragma unroll
  for (int k = 0; k < 2; ++k) t[k] = swap16_add(r[k], r[k + 2]);
  lds_st<v2u>(mg + w * 512 + (8 * c16 + 4 * (g >> 1) + 2 * (g & 1)) * 4, v2u{__float_as_uint(t[0]), __float_as_uint(t[1])});
  if (lane == 0) lds_st<v2u>(ST + w * 8, v2u{__float_as_uint(m), __float_as_uint(l)});
  wg_barrier();
  if (tid < 192) {
    const f32x4 s0 = lds_ld<f32x4>(ST), s1 = lds_ld<f32x4>(ST + 16), s2 = lds_ld<f32x4>(ST + 32), s3 = lds_ld<f32x4>(ST + 48);
    const float M = fmaxf(fmaxf(fmaxf(s0[0], s0[2]), fmaxf(s1[0], s1[2])), fmaxf(fmaxf(s2[0], s2[2]), fmaxf(s3[0], s3[2])));
    float e[8];
    e[0] = __builtin_amdgcn_exp2f((s0[0] - M) * LOG2E); e[1] = __builtin_amdgcn_exp2f((s0[2] - M) * LOG2E);
    e[2] = __builtin_amdgcn_exp2f((s1[0] - M) * LOG2E); e[3] = __builtin_amdgcn_exp2f((s1[2] - M) * LOG2E);
    e[4] = __builtin_amdgcn_exp2f((s2[0] - M) * LOG2E); e[5] = __builtin_amdgcn_exp2f((s2[2] - M) * LOG2E);
    e[6] = __builtin_amdgcn_exp2f((s3[0] - M) * LOG2E); e[7] = __builtin_amdgcn_exp2f((s3[2] - M) * LOG2E);
    if (tid < 128) {
      float o = 0.f;
#pragma unroll
      for (int ww = 0; ww < 8; ++ww) o = fmaf(e[ww], lds_ld<float>(mg + ww * 512 + tid * 4), o);
      st8(rs, rec + tid * 8, g_lo(o, tg), tg.hi);
    } else if (tid == 128) {
      const float L = e[0] * s0[1] + e[1] * s0[3] + e[2] * s1[1] + e[3] * s1[3] + e[4] * s2[1] + e[5] * s2[3] + e[6] * s3[1] + e[7] * s3[3];
      st16(rs, rec + 1024, v4u{g_lo(L, tg), tg.hi, g_lo(M, tg), tg.hi});
    }
  }
}

DI void stream_wg(const Params& p, rsrc_t rs, const u64 E, int h, int j, unsigned lb) {
  const int b = j >> 1, half = j & 1, bh = b * 32 + h;
  const int tid = threadIdx.x, lane = tid & 63, w = __builtin_amdgcn_readfirstlane(tid >> 6), c16 = lane & 15, g = lane >> 4;
  const size_t off = ((size_t)bh * 8192 + 6144 + half * 1024 + w * (4 * NY)) * 128;
  const rsrc_t rk = mk_rsrc(p.kc + off, NY * 1024), rv = mk_rsrc(p.vc + off, NY * 1024);
  dma_slice(p.wqB + (size_t)(128 * j) * 4096 + h * 128, 4096, lb + 32768, w, lane);
  bf16x8 xa[4];
#pragma unroll
  for (int s = 0; s < 4; ++s) xa[s] = *reinterpret_cast<const bf16x8*>(p.xB + (lane & 1) * 512 + 128 * j + 32 * s + 8 * g);
  v4u K[NY], V[NY];
#pragma unroll
  for (int i = 0; i < KINIT; ++i) K[i] = ldnt_b(rk, lane * 16, i * 1024);
  asm volatile("s_waitcnt vmcnt(%0)" :: "i"(KINIT) : "memory");
  wg_barrier();
  const Tag tg = make_tag(E);
  {
    const f32x4v d = gemv_mfma(xa, lb + 32768, w, lane);
    gemv_publish(rs, tg, d, WS_Q1 + (((unsigned)h * 4 + j) * 2 + 1) * 2048, 128, w, lane);
  }
  v4u qa[4];
  const unsigned qoff = WS_Q1 + ((((unsigned)h * 4) * 2 + 1) * 2 + b) * 1024 + lane * 16;
#pragma unroll
  for (int i = KINIT; i < 2 * NY; ++i) {
    if (i < NY) K[i] = ldnt_b(rk, lane * 16, i * 1024); else V[i - NY] = ldnt_b(rv, lane * 16, (i - NY) * 1024);
    if (i == NY - 4) { if (w == 0) {
#pragma unroll
        for (int pj = 0; pj < 4; ++pj) qa[pj] = ld16(rs, qoff + pj * 4096); } }
    if (i == NY + 4) {
      if (w == 0) {
        unsigned spins = 0;
        while (!__all(g_ok2(qa[0], tg) && g_ok2(qa[1], tg) && g_ok2(qa[2], tg) && g_ok2(qa[3], tg)) && ++spins < SPIN_CAP) {
          asm volatile("" ::: "memory");
#pragma unroll
          for (int pj = 0; pj < 4; ++pj) qa[pj] = ld16(rs, qoff + pj * 4096);
        }
        const float q0 = (g_val(qa[0].x) + g_val(qa[1].x)) + (g_val(qa[2].x) + g_val(qa[3].x));
        const float q1 = (g_val(qa[0].z) + g_val(qa[1].z)) + (g_val(qa[2].z) + g_val(qa[3].z));
        lds_st<unsigned>(lb + lane * 4, pk_bf(q0, q1));
      }
    }
    if (i < KINIT + KSLOW) asm volatile("s_waitcnt vmcnt(%0)" :: "i"(SWIN) : "memory");
    else if (i >= KWIN) asm volatile("s_waitcnt vmcnt(%0)" :: "i"(KWIN) : "memory");
  }
  wg_barrier();
  {
    const v4u q = lds_ld<v4u>(lb + c16 * 16);
    float s[NY], m = -INFINITY;
#pragma unroll
    for (int i = 0; i < NY; ++i) { s[i] = score4(K[i], q); m = fmaxf(m, s[i]); }
    pv_record<NY>(rs, tg, s, V, m, lb + 512, lb + 1024, WS_REC + (((unsigned)h * 4 + j) * 2 + 0) * 1088, tid);
  }
  dma_slice(p.wuv + (size_t)(128 * j) * 4096 + h * 128, 4096, lb + 65536, w, lane);
  dma_slice(p.woA + (size_t)(h * 128) * 512 + 128 * j, 512, lb + 98304, w, lane);
  const unsigned MLS = lb + 6144, V16 = lb + 7168, R2 = lb + 8192;
  if (tid < 128) {
    const v4u v = poll_pair(rs, WS_ML + ((unsigned)h * 128 + tid) * 16, tg);
    lds_st<v2u>(MLS + tid * 8, v2u{v.x << 8, v.z << 8});
  }
  wg_barrier();
  {
    v4u pv[4];
#pragma unroll
    for (int i = 0; i < 4; ++i) pv[i] = ld16(rs, WS_PART + (((unsigned)(16 * w + 4 * i + g) * 32 + h) * 512 + 128 * j + 8 * c16) * 2);
    const int bb = w >> 2;
    const v2u ml = lds_ld<v2u>(MLS + (bb * 64 + lane) * 8);
    const float m = __uint_as_float(ml.x), l = __uint_as_float(ml.y);
    float M = m;
    M = fmaxf(M, dpp<0xB1>(M)); M = fmaxf(M, dpp<0x4E>(M)); M = fmaxf(M, dpp<0x141>(M)); M = fmaxf(M, dpp<0x140>(M)); M = xrow_max(M);
    const float e = __builtin_amdgcn_exp2f((m - M) * LOG2E);
    const float L = xrow_sum(row16_sum(e * l));
    const float wt = e / L;
    float wi[4];
#pragma unroll
    for (int i = 0; i < 4; ++i) wi[i] = __shfl(wt, (16 * w + 4 * i + g) & 63);
    float acc[8];
#pragma unroll
    for (int k = 0; k < 8; ++k) acc[k] = 0.f;
#pragma unroll
    for (int i = 0; i < 4; ++i) {
#pragma unroll
      for (int k = 0; k < 4; ++k) { acc[2 * k] = fmaf(wi[i], bflo(pv[i][k]), acc[2 * k]); acc[2 * k + 1] = fmaf(wi[i], bfhi(pv[i][k]), acc[2 * k + 1]); }
    }
    float r[4], t[2];
#pragma unroll
    for (int k = 0; k < 4; ++k) r[k] = swap32_add(acc[k], acc[k + 4]);
#pragma unroll
    for (int k = 0; k < 2; ++k) t[k] = swap16_add(r[k], r[k + 2]);
    lds_st<v2u>(R2 + (w * 128 + 8 * c16 + 4 * (g >> 1) + 2 * (g & 1)) * 4, v2u{__float_as_uint(t[0]), __float_as_uint(t[1])});
  }
  wg_barrier();
  if (tid < 128) {
    const int bb = tid >> 6, cp = tid & 63;
    float x0 = 0.f, x1 = 0.f;
#pragma unroll
    for (int ww = 0; ww < 4; ++ww) { const v2u t = lds_ld<v2u>(R2 + ((4 * bb + ww) * 128 + 2 * cp) * 4); x0 += __uint_as_float(t.x); x1 += __uint_as_float(t.y); }
    lds_st<unsigned>(V16 + tid * 4, pk_bf(x0, x1));
  }
  wg_barrier();
  {
    bf16x8 f[4];
    vec_frags(f, V16, lane);
    const f32x4v d = gemv_mfma(f, lb + 65536, w, lane);
    gemv_publish(rs, tg, d, WS_C4 + ((unsigned)h * 4 + j) * 2048, 128, w, lane);
  }
  if (tid < 128) {
    const unsigned v = gather4(rs, tg, WS_C4 + ((unsigned)h * 4) * 2048 + (tid >> 6) * 1024 + (tid & 63) * 16, 2048);
    lds_st<unsigned>(V16 + 512 + tid * 4, v);
  }
  wg_barrier();
  {
    bf16x8 f[4];
    vec_frags(f, V16 + 512, lane);
    const f32x4v d = gemv_mfma(f, lb + 98304, w, lane);
    gemv_publish(rs, tg, d, WS_Y + (((unsigned)0 * 32 + h) * 1024 + 128 * j) * 8, 512, w, lane);
  }
  if (w == 0) final_reduce(p, rs, tg, 0, h * 4 + j, lane);
}

DI void chain_wg(const Params& p, rsrc_t rs, const u64 E, int a, unsigned lb) {
  const int h = a >> 2, j = a & 3;
  const int tid = threadIdx.x, lane = tid & 63, w = __builtin_amdgcn_readfirstlane(tid >> 6), g = lane >> 4, c16 = lane & 15;
  const unsigned scr = lb + SCR;
  dma_slice(p.wqA + (size_t)(128 * j) * 4096 + h * 128, 4096, scr, w, lane);
  bf16x8 fa[4], WK[4];
#pragma unroll
  for (int s = 0; s < 4; ++s) fa[s] = *reinterpret_cast<const bf16x8*>(p.xA + (lane & 1) * 512 + 128 * j + 32 * s + 8 * g);
  {
    const rsrc_t rw = mk_rsrc(p.wuk + (size_t)(128 * j + 16 * w) * 4096 + h * 128, 16 * 8192);
#pragma unroll
    for (int s = 0; s < 4; ++s) WK[s] = __builtin_bit_cast(bf16x8, ldnt_b(rw, c16 * 8192 + g * 16, 64 * s));
  }
  const int bA = a >> 6;
  asm volatile("s_barrier" ::: "memory");
  dma_rows1k<0, CK0>(p.ckv + (size_t)a * 128 * 512, lb, w, lane);
  asm volatile("s_waitcnt vmcnt(%0)" :: "i"(4 + CK0) : "memory");
  wg_barrier();
  const Tag tg = make_tag(E);
  {
    const f32x4v d = gemv_mfma(fa, scr, w, lane);
    gemv_publish(rs, tg, d, WS_Q1 + (((unsigned)h * 4 + j) * 2 + 0) * 2048, 128, w, lane);
  }
  dma_rows1k<CK0, CK1>(p.ckv + (size_t)a * 128 * 512, lb, w, lane);
  wg_barrier();
  if (tid < 128) {
    const unsigned v = gather4(rs, tg, WS_Q1 + (((unsigned)h * 4) * 4 + (tid >> 6)) * 1024 + (tid & 63) * 16, 4096);
    lds_st<unsigned>(scr + tid * 4, v);
  } else if (tid < 192) {
    const unsigned v = gather4(rs, tg, WS_Q1 + (((unsigned)h * 4) * 4 + 2 + (j >> 1)) * 1024 + lane * 16, 4096);
    lds_st<unsigned>(scr + 512 + lane * 4, v);
  }
  wg_barrier();
  const v4u qB = lds_ld<v4u>(scr + 512 + c16 * 16);
  {
    bf16x8 qf[4];
    vec_frags(qf, scr, lane);
    f32x4v d = {0.f, 0.f, 0.f, 0.f};
#pragma unroll
    for (int s = 0; s < 4; ++s) d = __builtin_amdgcn_mfma_f32_16x16x32_bf16(WK[s], qf[s], d, 0, 0, 0);
    if (c16 < 2)
      st16(rs, WS_QL + (((unsigned)c16 * 32 + h) * 256 + 64 * j + 8 * w + 2 * g) * 8, v4u{pk_bf(d[0], d[1]), tg.t32, pk_bf(d[2], d[3]), tg.t32});
  }
  dma_rows1k<CK1, 16>(p.ckv + (size_t)a * 128 * 512, lb, w, lane);
  const int r32 = lane & 31, hh = lane >> 5;
  float sc[8];
  s16x4 xt[32];
  bf16x8 ka[16];
  const unsigned fr = swz(r32);
  {
    v4u v[8];
    const unsigned base = WS_QL + (unsigned)bA * 65536 + tid * 16;
#pragma unroll
    for (int i = 0; i < 8; ++i) { v[i] = ld16(rs, base + i * 8192); }
    asm volatile("s_waitcnt vmcnt(8)" ::: "memory");
    wg_barrier();
    {
      const unsigned abase = lb + (32 * (w & 3) + r32) * 1024 + 512 * (w >> 2);
#pragma unroll
      for (int s = 0; s < 16; ++s) ka[s] = lds_ld<bf16x8>(abase + 16 * ((unsigned)((2 * s + hh) & 15) ^ fr) + 256 * (s >> 3));
      const unsigned blk = (lane >> 4) & 1, q = (lane & 15) >> 2, pp = lane & 3;
      unsigned tb[4];
#pragma unroll
      for (int mt = 0; mt < 2; ++mt)
#pragma unroll
        for (int t = 0; t < 2; ++t) {
          const unsigned row = 8 * hh + 4 * t + q, chn = 8 * w + 4 * mt + 2 * blk + (pp >> 1);
          tb[mt * 2 + t] = lb + row * 1024 + 16 * (chn ^ swz(row)) + 8 * (pp & 1);
        }
#pragma unroll
      for (int hf = 0; hf < 2; ++hf) {
        asm volatile(
            "ds_read_b64_tr_b16 %0, %16\n\tds_read_b64_tr_b16 %1, %17\n\tds_read_b64_tr_b16 %2, %18\n\tds_read_b64_tr_b16 %3, %19\n\t"
            "ds_read_b64_tr_b16 %4, %16 offset:16384\n\tds_read_b64_tr_b16 %5, %17 offset:16384\n\tds_read_b64_tr_b16 %6, %18 offset:16384\n\tds_read_b64_tr_b16 %7, %19 offset:16384\n\t"
            "ds_read_b64_tr_b16 %8, %16 offset:32768\n\tds_read_b64_tr_b16 %9, %17 offset:32768\n\tds_read_b64_tr_b16 %10, %18 offset:32768\n\tds_read_b64_tr_b16 %11, %19 offset:32768\n\t"
            "ds_read_b64_tr_b16 %12, %16 offset:49152\n\tds_read_b64_tr_b16 %13, %17 offset:49152\n\tds_read_b64_tr_b16 %14, %18 offset:49152\n\tds_read_b64_tr_b16 %15, %19 offset:49152\n\t"
            "s_waitcnt lgkmcnt(0)"
            : "=&v"(xt[16 * hf + 0]), "=&v"(xt[16 * hf + 1]), "=&v"(xt[16 * hf + 2]), "=&v"(xt[16 * hf + 3]),
              "=&v"(xt[16 * hf + 4]), "=&v"(xt[16 * hf + 5]), "=&v"(xt[16 * hf + 6]), "=&v"(xt[16 * hf + 7]),
              "=&v"(xt[16 * hf + 8]), "=&v"(xt[16 * hf + 9]), "=&v"(xt[16 * hf + 10]), "=&v"(xt[16 * hf + 11]),
              "=&v"(xt[16 * hf + 12]), "=&v"(xt[16 * hf + 13]), "=&v"(xt[16 * hf + 14]), "=&v"(xt[16 * hf + 15])
            : "v"(tb[0] + hf * 65536), "v"(tb[1] + hf * 65536), "v"(tb[2] + hf * 65536), "v"(tb[3] + hf * 65536) : "memory");
      }
    }
    unsigned spins = 0;
    const unsigned cq = tid & 127, h0 = tid >> 7;
    for (;;) {
      bool bad[8], any = false;
#pragma unroll
      for (int i = 0; i < 8; ++i) { bad[i] = !__all(v[i].y == tg.t32 && v[i].w == tg.t32); any = any || bad[i]; }
      if (!any || ++spins >= SPIN_CAP) break;
      asm volatile("" ::: "memory");
#pragma unroll
      for (int i = 0; i < 8; ++i) if (bad[i]) v[i] = ld16(rs, base + i * 8192);
    }
#pragma unroll
    for (int i = 0; i < 8; ++i) {
      const unsigned head = 4 * i + h0;
      lds_st<v2u>(scr + head * 1024 + 16 * ((cq >> 1) ^ swz(head)) + 8 * (cq & 1), v2u{v[i].x, v[i].z});
    }
  }
  wg_barrier();
  const size_t koff = ((size_t)((j >> 1) * 32 + h) * 8192 + 6144 + (j & 1) * 1024 + 32 * NY + w * (4 * NX)) * 128;
  const rsrc_t rk = mk_rsrc(p.kc + koff, NX * 1024), rv = mk_rsrc(p.vc + koff, NX * 1024);
  v4u K[NX], V[NX];
#pragma unroll
  for (int i = 0; i < 7; ++i) K[i] = ldnt_b(rk, lane * 16, i * 1024);
  {
    const int ch = w >> 2;
    f32x16 acc;
#pragma unroll
    for (int i = 0; i < 16; ++i) acc[i] = 0.f;
    const unsigned bbase = scr + r32 * 1024 + 512 * ch;
#pragma unroll
    for (int s = 0; s < 16; ++s) {
      const bf16x8 bv = lds_ld<bf16x8>(bbase + 16 * ((unsigned)((2 * s + hh) & 15) ^ fr) + 256 * (s >> 3));
      acc = __builtin_amdgcn_mfma_f32_32x32x16_bf16(ka[s], bv, acc, 0, 0, 0);
      if ((s & 7) == 7) __builtin_amdgcn_sched_barrier(0);
    }
    f32x4 s0, s1;
#pragma unroll
    for (int k = 0; k < 4; ++k) { s0[k] = ch ? acc[k] : acc[8 + k]; s1[k] = ch ? acc[4 + k] : acc[12 + k]; }
    const unsigned XB = lb + 32768;
    lds_st<f32x4>(XB + w * 2048 + lane * 16, s0);
    lds_st<f32x4>(XB + w * 2048 + 1024 + lane * 16, s1);
    wg_barrier();
    const f32x4 t0 = lds_ld<f32x4>(XB + (w ^ 4) * 2048 + lane * 16), t1 = lds_ld<f32x4>(XB + (w ^ 4) * 2048 + 1024 + lane * 16);
#pragma unroll
    for (int k = 0; k < 4; ++k) { sc[k] = ((ch ? acc[8 + k] : acc[k]) + t0[k]) * SCALE; sc[4 + k] = ((ch ? acc[12 + k] : acc[4 + k]) + t1[k]) * SCALE; }
  }
#pragma unroll
  for (int i = 7; i < NX; ++i) K[i] = ldnt_b(rk, lane * 16, i * 1024);
  const unsigned MX = scr + 16384, LS = scr + 17408, PB = scr + 18432;
  float Mh;
  {
    float m = sc[0];
#pragma unroll
    for (int k = 1; k < 8; ++k) m = fmaxf(m, sc[k]);
    { auto t = __builtin_amdgcn_permlane32_swap(__float_as_uint(m), __float_as_uint(m), false, false); m = fmaxf(__uint_as_float(t[0]), __uint_as_float(t[1])); }
    if (lane < 32) lds_st<float>(MX + (lane * 8 + w) * 4, m);
    wg_barrier();
    const f32x4 ma = lds_ld<f32x4>(MX + r32 * 32), mb = lds_ld<f32x4>(MX + r32 * 32 + 16);
    float M = fmaxf(fmaxf(fmaxf(ma[0], ma[1]), fmaxf(ma[2], ma[3])), fmaxf(fmaxf(mb[0], mb[1]), fmaxf(mb[2], mb[3])));
    M = round24(M);
    Mh = M;
    const float mneg = -M * LOG2E;
    unsigned pk[4]; float l = 0.f;
#pragma unroll
    for (int k = 0; k < 4; ++k) {
      const float p0 = __builtin_amdgcn_exp2f(fmaf(sc[2 * k], LOG2E, mneg)), p1 = __builtin_amdgcn_exp2f(fmaf(sc[2 * k + 1], LOG2E, mneg));
      pk[k] = pk_bf(p0, p1);
      l += bflo(pk[k]) + bfhi(pk[k]);
    }
    { auto t = __builtin_amdgcn_permlane32_swap(__float_as_uint(l), __float_as_uint(l), false, false); l = __uint_as_float(t[0]) + __uint_as_float(t[1]); }
    if (lane < 32) lds_st<float>(LS + (lane * 8 + w) * 4, l);
    const int kt = w & 3, ch = w >> 2;
    lds_st<v2u>(PB + r32 * 272 + (32 * kt + 16 * ch + 4 * hh) * 2, v2u{pk[0], pk[1]});
    lds_st<v2u>(PB + r32 * 272 + (32 * kt + 16 * ch + 8 + 4 * hh) * 2, v2u{pk[2], pk[3]});
  }
  wg_barrier();
  f32x16 o0, o1;
#pragma unroll
  for (int i = 0; i < 16; ++i) { o0[i] = 0.f; o1[i] = 0.f; }
#pragma unroll
  for (int s = 0; s < 8; ++s) {
    const bf16x8 pv = lds_ld<bf16x8>(PB + r32 * 272 + 32 * s + 16 * hh);
    o0 = __builtin_amdgcn_mfma_f32_32x32x16_bf16(__builtin_shufflevector(xt[4 * s], xt[4 * s + 1], 0, 1, 2, 3, 4, 5, 6, 7), pv, o0, 0, 0, 0);
    o1 = __builtin_amdgcn_mfma_f32_32x32x16_bf16(__builtin_shufflevector(xt[4 * s + 2], xt[4 * s + 3], 0, 1, 2, 3, 4, 5, 6, 7), pv, o1, 0, 0, 0);
  }
  {
    const unsigned tb = lb + 32768 + w * 4096;
#pragma unroll
    for (int gq = 0; gq < 4; ++gq) {
      lds_st<v2u>(tb + r32 * 128 + 16 * (gq ^ (r32 & 7)) + 8 * hh, v2u{pk_bf(o0[4 * gq], o0[4 * gq + 1]), pk_bf(o0[4 * gq + 2], o0[4 * gq + 3])});
      lds_st<v2u>(tb + r32 * 128 + 16 * ((4 + gq) ^ (r32 & 7)) + 8 * hh, v2u{pk_bf(o1[4 * gq], o1[4 * gq + 1]), pk_bf(o1[4 * gq + 2], o1[4 * gq + 3])});
    }
    asm volatile("s_waitcnt lgkmcnt(0)" ::: "memory");
#pragma unroll
    for (int i = 0; i < 4; ++i) {
      const int head = 8 * i + (lane >> 3);
      const v4u v = lds_ld<v4u>(tb + head * 128 + 16 * ((lane & 7) ^ (head & 7)));
      st16(rs, WS_PART + (((unsigned)a * 32 + head) * 512 + 64 * w + 8 * (lane & 7)) * 2, v);
    }
    asm volatile("" ::: "memory");
#pragma unroll
    for (int i = 0; i < 6; ++i) V[i] = ldnt_b(rv, lane * 16, i * 1024);
    asm volatile("s_waitcnt vmcnt(6)" ::: "memory");
  }
  wg_barrier();
  if (w == 0 && lane < 32) {
    const f32x4 la = lds_ld<f32x4>(LS + lane * 32), lc = lds_ld<f32x4>(LS + lane * 32 + 16);
    const float l = ((la[0] + la[1]) + (la[2] + la[3])) + ((lc[0] + lc[1]) + (lc[2] + lc[3]));
    st16(rs, WS_ML + ((unsigned)lane * 128 + a) * 16, v4u{g_lo(Mh, tg), tg.hi, g_lo(l, tg), tg.hi});
  }
  if (a == 0 && w == 7) {
    v4u d0 = ld16(rs, WS_Q1 + ((unsigned)lane * 2 + 1) * 2048), d1 = ld16(rs, WS_Q1 + ((unsigned)(lane + 64) * 2 + 1) * 2048);
    unsigned spins = 0;
    while (!__all(g_ok2(d0, tg) && g_ok2(d1, tg)) && ++spins < SPIN_CAP) {
      __builtin_amdgcn_s_sleep(2); asm volatile("" ::: "memory");
      d0 = ld16(rs, WS_Q1 + ((unsigned)lane * 2 + 1) * 2048); d1 = ld16(rs, WS_Q1 + ((unsigned)(lane + 64) * 2 + 1) * 2048);
    }
    if (lane == 0) __hip_atomic_store(reinterpret_cast<u64*>(p.ws + WS_EPOCH), ((u64)tg.top << 32) | tg.t32, __ATOMIC_RELAXED, __HIP_MEMORY_SCOPE_AGENT);
  }
  dma_slice(p.woB + (size_t)(h * 128) * 512 + 128 * j, 512, lb, w, lane);
#pragma unroll
  for (int i = 6; i < NX; ++i) V[i] = ldnt_b(rv, lane * 16, i * 1024);
  {
    float s[NX], m = -INFINITY;
#pragma unroll
    for (int i = 0; i < NX; ++i) { s[i] = score4(K[i], qB); m = fmaxf(m, s[i]); }
    pv_record<NX>(rs, tg, s, V, m, lb + 65536, lb + 66560, WS_REC + (((unsigned)h * 4 + j) * 2 + 1) * 1088, tid);
  }
  const unsigned V16 = lb + 71680;
  if (tid < 128) {
    const int bb = tid >> 6, dp = tid & 63;
    const unsigned r0 = WS_REC + (((unsigned)h * 4 + 2 * bb) * 2) * 1088;
    v4u o[4], st[4];
#pragma unroll
    for (int r = 0; r < 4; ++r) { o[r] = ld16(rs, r0 + r * 1088 + dp * 16); st[r] = ld16(rs, r0 + r * 1088 + 1024); }
    unsigned spins = 0;
    for (;;) {
      bool bad[4], any = false;
#pragma unroll
      for (int r = 0; r < 4; ++r) { bad[r] = !__all(g_ok2(o[r], tg) && g_ok2(st[r], tg)); any = any || bad[r]; }
      if (!any || ++spins >= SPIN_CAP) break;
      asm volatile("" ::: "memory");
#pragma unroll
      for (int r = 0; r < 4; ++r) if (bad[r]) { o[r] = ld16(rs, r0 + r * 1088 + dp * 16); st[r] = ld16(rs, r0 + r * 1088 + 1024); }
    }
    const float Mx = fmaxf(fmaxf(g_val(st[0].z), g_val(st[1].z)), fmaxf(g_val(st[2].z), g_val(st[3].z)));
    float L = 0.f, o0 = 0.f, o1 = 0.f;
#pragma unroll
    for (int r = 0; r < 4; ++r) {
      const float e = __builtin_amdgcn_exp2f((g_val(st[r].z) - Mx) * LOG2E);
      L = fmaf(e, g_val(st[r].x), L); o0 = fmaf(e, g_val(o[r].x), o0); o1 = fmaf(e, g_val(o[r].z), o1);
    }
    const float inv = 1.f / L;
    lds_st<unsigned>(V16 + tid * 4, pk_bf(o0 * inv, o1 * inv));
  }
  asm volatile("s_waitcnt vmcnt(0)" ::: "memory");
  wg_barrier();
  {
    bf16x8 f[4];
    vec_frags(f, V16, lane);
    const f32x4v d = gemv_mfma(f, lb, w, lane);
    gemv_publish(rs, tg, d, WS_Y + (((unsigned)1 * 32 + h) * 1024 + 128 * j) * 8, 512, w, lane);
  }
  if (w == 0) final_reduce(p, rs, tg, 1, a, lane);
}

__global__ void __launch_bounds__(512, 2) fused_decode_kernel(Params p) {
  extern __shared__ __attribute__((aligned(16))) char smem[];
  const unsigned lb = (unsigned)(uintptr_t)(LAS char*)smem;
  asm volatile("" :: "s"(p.xA), "s"(p.wqA), "s"(p.wqB), "s"(p.ws));
  u64 pc; asm volatile("s_getpc_b64 %0" : "=s"(pc));
  const rsrc_t rs = __builtin_amdgcn_make_buffer_rsrc(p.ws, 0, WS_END, 0x00020000);
  const u64 E = __hip_atomic_load(reinterpret_cast<u64*>(p.ws + WS_EPOCH), __ATOMIC_RELAXED, __HIP_MEMORY_SCOPE_AGENT);
  const int gidx = blockIdx.x;
  const int role = (gidx >> 3) & 1, k = gidx >> 4, head = 4 * (gidx & 7) + (k >> 2), j = k & 3;
  if (role == 0) {
    const unsigned o = (threadIdx.x * 64u < CODE_BYTES) ? threadIdx.x * 64u : 0u;
    const u64 ad = (pc & ~63ull) + o;
    unsigned keep;
    asm volatile("s_mov_b32 %0, m0\n\ts_mov_b32 m0, %2\n\ts_nop 0\n\tglobal_load_lds_dword %1, off\n\ts_mov_b32 m0, %0"
                 : "=&s"(keep) : "v"(ad), "s"(lb + 16384u + (unsigned)__builtin_amdgcn_readfirstlane(threadIdx.x >> 6) * 256u) : "memory");
    stream_wg(p, rs, E, head, j, lb);
  }
  else chain_wg(p, rs, E, head * 4 + j, lb);
}

extern "C" void kernel_launch(void* const* d_in, const int* in_sizes, int n_in, void* d_out, int out_size, void* d_ws, size_t ws_size,
                              hipStream_t stream) {
  Params p;
  p.xA = (const u16*)d_in[0]; p.ckv = (const u16*)d_in[1]; p.wqA = (const u16*)d_in[2]; p.wuk = (const u16*)d_in[3];
  p.wuv = (const u16*)d_in[4]; p.woA = (const u16*)d_in[5]; p.xB = (const u16*)d_in[6]; p.kc = (const u16*)d_in[7];
  p.vc = (const u16*)d_in[8]; p.wqB = (const u16*)d_in[9]; p.woB = (const u16*)d_in[10];
  p.out = (u16*)d_out; p.ws = (unsigned char*)d_ws;
  (void)hipFuncSetAttribute((const void*)fused_decode_kernel, hipFuncAttributeMaxDynamicSharedMemorySize, LDS_BYTES);
  fused_decode_kernel<<<256, 512, LDS_BYTES, stream>>>(p);
}
```
